```python
import torch

def _softmax(x, axis=-1):
    return torch.softmax(x, dim=axis)
def _gelu(x):
    return torch.nn.functional.gelu(x, approximate="tanh")
def _silu(x):
    return torch.nn.functional.silu(x)
def _sigmoid(x):
    return torch.sigmoid(x)
def _layernorm(x, axis=-1, eps=1e-5):
    m = x.mean(dim=axis, keepdim=True)
    v = x.var(dim=axis, keepdim=True, unbiased=False)
    return (x - m) / torch.sqrt(v + eps)
def _scatter_add(tgt, idx, src, axis=0):
    return tgt.clone().index_add_(axis, idx, src)
def _segment_sum(src, seg, n):
    out = torch.zeros((n,) + src.shape[1:], dtype=src.dtype)
    return out.index_add_(0, seg, src)
def _pad(x, pw):
    flat = tuple(v for pair in reversed(pw) for v in pair)
    return torch.nn.functional.pad(x, flat)

kernel_name = 'gated_mix__ssm_conv_s4d__ssm_conv_h3__prompt'

def setup_inputs():

    _w = (lambda a: (lambda t: t.to(torch.bfloat16) if t.is_floating_point() else t)(torch.as_tensor(a)))
    def _A_setup():
        Bsz, L, D, N = 1, 2048, 512, 128
        return {
            "u": _w((lambda *s: torch.randn(*s, dtype=torch.bfloat16) * 0.05)(Bsz, L, D)),
            "log_A_real": _w((lambda *s: torch.randn(*s, dtype=torch.bfloat16) * 3.2)(D, N) * 0.5),
            "A_imag": _w((lambda *s: torch.randn(*s, dtype=torch.bfloat16) * 0.2)(D, N) * 3.14),
            "C_real": _w((lambda *s: torch.randn(*s, dtype=torch.bfloat16) * 3.2)(D, N) / N ** 0.5),
            "C_imag": _w((lambda *s: torch.randn(*s, dtype=torch.bfloat16) * 3.2)(D, N) / N ** 0.5),
            "log_dt": _w((lambda *s: torch.randn(*s, dtype=torch.bfloat16) * 0.2)(D) * 0.5 - 4.0),
            "D_skip": _w((lambda *s: torch.randn(*s, dtype=torch.bfloat16) * 0.4)(D)),
        }
    _A_in = _A_setup()

    def _B_setup():
        B, S, D = 1, 2048, 512
        ks = 4
        return {
            "q": _w((lambda *s: torch.randn(*s, dtype=torch.bfloat16) * 0.05)(B, S, D)),
            "k": _w((lambda *s: torch.randn(*s, dtype=torch.bfloat16) * 0.8)(B, S, D)),
            "v": _w((lambda *s: torch.randn(*s, dtype=torch.bfloat16) * 0.8)(B, S, D)),
            "shift_k": _w((lambda *s: torch.randn(*s, dtype=torch.bfloat16) * 0.8)(D, ks) * 0.5),
            "ssm_k": _w((lambda *s: torch.randn(*s, dtype=torch.bfloat16) * 0.8)(D, S) * (1.0 / S ** 0.5)),
            "ssm_D": _w((lambda *s: torch.randn(*s, dtype=torch.bfloat16) * 0.8)(D)),
        }
    _B_in = _B_setup()

    return {
        "A_u": _A_in["u"],
        "A_log_A_real": _A_in["log_A_real"],
        "A_A_imag": _A_in["A_imag"],
        "A_C_real": _A_in["C_real"],
        "A_C_imag": _A_in["C_imag"],
        "A_log_dt": _A_in["log_dt"],
        "A_D_skip": _A_in["D_skip"],
        "B_q": _B_in["q"],
        "B_k": _B_in["k"],
        "B_v": _B_in["v"],
        "B_shift_k": _B_in["shift_k"],
        "B_ssm_k": _B_in["ssm_k"],
        "B_ssm_D": _B_in["ssm_D"],
        "gate_w": _w((lambda *s: torch.randn(*s, dtype=torch.bfloat16) * 0.05)(512) * 16.0),
    }

def reference(A_u, A_log_A_real, A_A_imag, A_C_real, A_C_imag, A_log_dt, A_D_skip, B_q, B_k, B_v, B_shift_k, B_ssm_k, B_ssm_D, gate_w):
    def _A_ref(u, log_A_real, A_imag, C_real, C_imag, log_dt, D_skip):
        L = u.shape[1]
        dt = torch.exp(log_dt)
        A = -torch.exp(log_A_real) + 1j * A_imag
        C = (C_real + 1j * C_imag) * dt[:, None]
        pos = (lambda x, dt: x.to(dt))(torch.arange(L), log_dt.dtype)
        P = torch.exp((A * dt[:, None])[:, :, None] * pos[None, None, :])
        K = 2.0 * torch.einsum("dn,dnl->dl", C, P).real
        L2 = 2 * L
        u_t = (lambda x, *d: x.permute(*d))(u, 0, 2, 1)
        Kf = (lambda x, *a, **k: torch.fft.rfft(x.float(), *a, **k))(K, L2)
        Uf = (lambda x, *a, **k: torch.fft.rfft(x.float(), *a, **k))(u_t, L2)
        y = (lambda x, *a, **k: torch.fft.irfft(x, *a, **k).to(torch.bfloat16))(Uf * Kf[None, :, :], L2)[:, :, :L]
        y = (lambda x, *d: x.permute(*d))(y, 0, 2, 1)
        return y + u * D_skip[None, None, :]

    def _B_ref(q, k, v, shift_k, ssm_k, ssm_D):
        B, S, D = q.shape
        ks = shift_k.shape[-1]
        k_pad = _pad(k, ((0, 0), (ks - 1, 0), (0, 0)))
        k_shift = torch.zeros_like(k)
        for t in range(ks):
            k_shift = k_shift + k_pad[:, t:t + S, :] * shift_k[:, ks - 1 - t][None, None, :]
        kv = k_shift * v
        L = 2 * S
        kv_pad = _pad(kv, ((0, 0), (0, S), (0, 0)))
        kf_pad = _pad(ssm_k, ((0, 0), (0, S)))
        kv_f = (lambda x, axes=(-2,-1): torch.fft.rfft2(x.float(), dim=axes))(kv_pad[:, None, :, :], axes=(1, 2))
        kf_f = (lambda x, axes=(-2,-1): torch.fft.rfft2(x.float(), dim=axes))(kf_pad[:, None, :], axes=(1, 2))
        kf_f = (lambda x, *d: x.permute(*d))(kf_f, 1, 2, 0)
        y_f = kv_f * kf_f[None, :, :, :]
        y = (lambda x, s, axes=(-2,-1): torch.fft.irfft2(x, s=s, dim=axes).to(torch.bfloat16))(y_f, (1, L), axes=(1, 2))[:, 0, :S, :]
        y = y + kv * ssm_D[None, None, :]
        return q * y

    x = A_u
    g = _sigmoid((x * gate_w).sum(-1, keepdims=True))
    a = _A_ref(x, A_log_A_real, A_A_imag, A_C_real, A_C_imag, A_log_dt, A_D_skip)
    b = _B_ref(x, B_k, B_v, B_shift_k, B_ssm_k, B_ssm_D)
    return g * a + (1 - g) * b
```

```cpp
#include <hip/hip_runtime.h>
#include <hip/hip_bf16.h>
#include <stdint.h>

#define L_ 2048
#define D_ 512
#define N_ 128

typedef unsigned short u16;
__device__ __forceinline__ float bf2f(u16 x) { return __uint_as_float(((unsigned)x) << 16); }
__device__ __forceinline__ u16 f2bf(float f) {
    __hip_bfloat16 b = __float2bfloat16(f);
    return *reinterpret_cast<u16*>(&b);
}
__device__ __forceinline__ float rbf(float f) { return bf2f(f2bf(f)); }

__global__ void kgen(const u16* lar, const u16* aim, const u16* cr, const u16* ci, const u16* ldt, float* K) {
    int d = blockIdx.x;
    __shared__ float zr[N_], zi[N_], c0[N_], c1[N_];
    float dt = rbf(expf(bf2f(ldt[d])));
    if (threadIdx.x < N_) {
        int n = threadIdx.x;
        float ar = -rbf(expf(bf2f(lar[d * N_ + n])));
        zr[n] = ar * dt;
        zi[n] = bf2f(aim[d * N_ + n]) * dt;
        c0[n] = bf2f(cr[d * N_ + n]) * dt;
        c1[n] = bf2f(ci[d * N_ + n]) * dt;
    }
    __syncthreads();
    for (int l = threadIdx.x; l < L_; l += blockDim.x) {
        float p = (float)l;
        float acc = 0.f;
        for (int n = 0; n < N_; ++n) {
            float e = expf(zr[n] * p);
            float s, c;
            sincosf(zi[n] * p, &s, &c);
            acc += c0[n] * (e * c) - c1[n] * (e * s);
        }
        K[d * L_ + l] = 2.f * acc;
    }
}

__global__ void prep(const u16* u, const u16* k, const u16* v, const u16* sk, const u16* gw, float* kv, float* g) {
    int l = blockIdx.x;
    __shared__ float red[8];
    int d = threadIdx.x;
    float ks = 0.f;
    for (int t = 0; t < 4; ++t) {
        int s = l + t - 3;
        float kk = s >= 0 ? bf2f(k[s * D_ + d]) : 0.f;
        ks = rbf(ks + rbf(kk * bf2f(sk[d * 4 + 3 - t])));
    }
    kv[l * D_ + d] = rbf(ks * bf2f(v[l * D_ + d]));
    float p = rbf(bf2f(u[l * D_ + d]) * bf2f(gw[d]));
    for (int m = 32; m >= 1; m >>= 1) p += __shfl_xor(p, m);
    if ((d & 63) == 0) red[d >> 6] = p;
    __syncthreads();
    if (d == 0) {
        float s = 0.f;
        for (int i = 0; i < 8; ++i) s += red[i];
        s = rbf(s);
        g[l] = rbf(1.f / (1.f + expf(-s)));
    }
}

__global__ void conv(const u16* u, const float* K, const float* kv, const u16* ssmk, const u16* dskip, const u16* ssmD,
                     const float* g, u16* out) {
    int d = blockIdx.x;
    __shared__ float su[L_], skv[L_], sK[L_], sS[L_];
    for (int l = threadIdx.x; l < L_; l += blockDim.x) {
        su[l] = bf2f(u[l * D_ + d]);
        skv[l] = kv[l * D_ + d];
        sK[l] = K[d * L_ + l];
        sS[l] = bf2f(ssmk[d * L_ + l]);
    }
    __syncthreads();
    float ds = bf2f(dskip[d]), sd = bf2f(ssmD[d]);
    for (int l = threadIdx.x; l < L_; l += blockDim.x) {
        float ya = 0.f, yb = 0.f;
        for (int j = 0; j <= l; ++j) {
            ya += sK[j] * su[l - j];
            yb += sS[j] * skv[l - j];
        }
        float a = rbf(rbf(ya) + rbf(su[l] * ds));
        float b = rbf(su[l] * rbf(rbf(yb) + rbf(skv[l] * sd)));
        float gg = g[l];
        out[l * D_ + d] = f2bf(rbf(gg * a) + rbf(rbf(1.f - gg) * b));
    }
}

extern "C" void kernel_launch(void* const* d_in, const int* in_sizes, int n_in, void* d_out, int out_size, void* d_ws,
                              size_t ws_size, hipStream_t stream) {
    const u16* u = (const u16*)d_in[0];
    const u16* lar = (const u16*)d_in[1];
    const u16* aim = (const u16*)d_in[2];
    const u16* cr = (const u16*)d_in[3];
    const u16* ci = (const u16*)d_in[4];
    const u16* ldt = (const u16*)d_in[5];
    const u16* dskip = (const u16*)d_in[6];
    const u16* k = (const u16*)d_in[8];
    const u16* v = (const u16*)d_in[9];
    const u16* sk = (const u16*)d_in[10];
    const u16* ssmk = (const u16*)d_in[11];
    const u16* ssmD = (const u16*)d_in[12];
    const u16* gw = (const u16*)d_in[13];
    float* K = (float*)d_ws;
    float* kv = K + D_ * L_;
    float* g = kv + D_ * L_;
    kgen<<<D_, 256, 0, stream>>>(lar, aim, cr, ci, ldt, K);
    prep<<<L_, 512, 0, stream>>>(u, k, v, sk, gw, kv, g);
    conv<<<D_, 1024, 0, stream>>>(u, K, kv, ssmk, dskip, ssmD, g, (u16*)d_out);
}
```

```cpp
#include <hip/hip_runtime.h>
#include <stdint.h>

#define FUSED 1

typedef unsigned short u16;
typedef unsigned v4u __attribute__((ext_vector_type(4)));
typedef unsigned v2u __attribute__((ext_vector_type(2)));
typedef short v4s __attribute__((ext_vector_type(4)));
typedef float v4f __attribute__((ext_vector_type(4)));
typedef float v2f __attribute__((ext_vector_type(2)));
typedef __bf16 v2bf __attribute__((ext_vector_type(2)));
typedef __bf16 v8bf __attribute__((ext_vector_type(8)));
typedef _Float16 v2h __attribute__((ext_vector_type(2)));
typedef _Float16 v8h __attribute__((ext_vector_type(8)));
#define LAS __attribute__((address_space(3)))
#define GAS __attribute__((address_space(1)))

constexpr int XR = 1184;
constexpr int XC = 4 * XR + 32;
constexpr int X_OFF = 0;
constexpr int KS = 4096 + 32;
constexpr int K_OFF = 19072;
constexpr int G_OFF = K_OFF + 4 * KS;
constexpr int T_OFF = G_OFF + 8192;
constexpr int TS_ = 1312;
constexpr int U_OFF = T_OFF + 2 * TS_;
constexpr int TROW = 136;
constexpr int TU_OFF = U_OFF;
constexpr int TV_OFF = TU_OFF + 64 * TROW;
constexpr int TK_OFF = TV_OFF + 64 * TROW;
constexpr int FG_OFF = TK_OFF + 9120;
constexpr int FGROW = 528;
constexpr int FGC = 53 * FGROW;
constexpr int FG_END = FG_OFF + 2 * FGC + 11 * FGROW;
constexpr int YS = 8192 + 64 + 16;
constexpr int Y_OFF = U_OFF;
constexpr int CS = 2048 + 16;
constexpr int C_OFF = Y_OFF + 8 * YS;
constexpr int LDS_BYTES = FG_END > C_OFF + 8 * CS ? FG_END : C_OFF + 8 * CS;

constexpr size_t WS_X = 0;
constexpr size_t WS_G = 8u << 20;
constexpr size_t WS_E = WS_G + 16384;
constexpr size_t WS_DIAG = WS_E + 4096;

__device__ __forceinline__ float bflo(unsigned x) { return __uint_as_float(x << 16); }
__device__ __forceinline__ float bfhi(unsigned x) { return __uint_as_float(x & 0xffff0000u); }
__device__ __forceinline__ unsigned pkbf(float a, float b) { v2f t = {a, b}; return __builtin_bit_cast(unsigned, __builtin_convertvector(t, v2bf)); }
__device__ __forceinline__ unsigned pkh(float a, float b) { v2f t = {a, b}; return __builtin_bit_cast(unsigned, __builtin_convertvector(t, v2h)); }
__device__ __forceinline__ float rbf(float a) { return bflo(pkbf(a, a)); }
__device__ __forceinline__ v2u trread(LAS char* p) { return __builtin_bit_cast(v2u, __builtin_amdgcn_ds_read_tr16_b64_v4i16((LAS v4s*)p)); }

struct cplx2 { v2f re, im; };
__device__ __forceinline__ cplx2 cmul(cplx2 a, cplx2 b) {
    cplx2 r;
    r.re = a.re * b.re - a.im * b.im;
    r.im = a.re * b.im + a.im * b.re;
    return r;
}
__device__ __forceinline__ cplx2 cexp_p(v2f zr, v2f zi, float p) {
    cplx2 r;
    float e0 = __builtin_amdgcn_exp2f(zr.x * p), e1 = __builtin_amdgcn_exp2f(zr.y * p);
    float t0 = __builtin_amdgcn_fractf(zi.x * p), t1 = __builtin_amdgcn_fractf(zi.y * p);
    r.re = (v2f){e0 * __builtin_amdgcn_cosf(t0), e1 * __builtin_amdgcn_cosf(t1)};
    r.im = (v2f){e0 * __builtin_amdgcn_sinf(t0), e1 * __builtin_amdgcn_sinf(t1)};
    return r;
}

#define NSTAMP 9
#define STAMP(i)

template <int MODE>
__global__ void __launch_bounds__(512, 2)
fused_kernel(const u16* __restrict__ u, const u16* __restrict__ lar, const u16* __restrict__ aim, const u16* __restrict__ cre,
             const u16* __restrict__ cim, const u16* __restrict__ ldt, const u16* __restrict__ dskip, const u16* __restrict__ kk,
             const u16* __restrict__ vv, const u16* __restrict__ shk, const u16* __restrict__ ssmk, const u16* __restrict__ ssmD,
             const u16* __restrict__ gw, u16* __restrict__ out, char* ws) {
    extern __shared__ __attribute__((aligned(16))) char smem_[];
    LAS char* sm = (LAS char*)smem_;
    const int tid = threadIdx.x, lane = tid & 63, wv = __builtin_amdgcn_readfirstlane(tid >> 6);
    const int gam = blockIdx.x & 7, m = blockIdx.x >> 3;
    const int d0 = 64 * gam + 2 * m;
    const __amdgpu_buffer_rsrc_t wsr = __builtin_amdgcn_make_buffer_rsrc(ws, 0, (int)(WS_E + 64), 0x00020000);
    STAMP(0);

    unsigned tag = 0x12345677u;
    v4u tu, tv, tk, th, gu_, gw_;
    v2u shv;
    if (MODE != 2) {
        const int row = tid >> 3, oct = tid & 7;
        const size_t off = (size_t)(64 * m + row) * 1024 + 128 * gam + 16 * oct;
        tu = *(const v4u*)((const char*)u + off);
        tk = *(const v4u*)((const char*)kk + off);
        tv = *(const v4u*)((const char*)vv + off);
        th = (v4u){0u, 0u, 0u, 0u};
        if (tid < 24 && m > 0) th = *(const v4u*)((const char*)kk + off - 3 * 1024);
        shv = *(const v2u*)(shk + (size_t)(64 * gam + 16 * (wv & 3) + (lane & 15)) * 4);
        if (MODE == 0) tag = __builtin_amdgcn_raw_buffer_load_b32(wsr, (int)WS_E, 0, 16) * 0x9E3779B1u + 0x7F4A7C15u;
    }
    unsigned p_lar, p_aim, p_cre, p_cim, p_ldt;
    v4u sk_;
    const int kc = wv >> 2, part = wv & 3;
    if (MODE != 1) {
        const int po = (d0 + kc) * 64 + lane;
        p_lar = ((const unsigned*)lar)[po];
        p_aim = ((const unsigned*)aim)[po];
        p_cre = ((const unsigned*)cre)[po];
        p_cim = ((const unsigned*)cim)[po];
        p_ldt = ldt[d0 + kc];
    }
    if (MODE != 2) {
        gu_ = *(const v4u*)((const char*)u + (size_t)(64 * m + 8 * gam + wv) * 1024 + 16 * lane);
        gw_ = *(const v4u*)((const char*)gw + 16 * lane);
    }
    if (MODE != 1) {
        sk_ = *(const v4u*)((const char*)ssmk + (size_t)d0 * 4096 + 16 * tid);
        if (tid < 16 * 8) { const int a = tid >> 3; *(LAS v4u*)(sm + X_OFF + (a >> 2) * XC + (a & 3) * XR + 16 * (tid & 7)) = (v4u){0u, 0u, 0u, 0u}; }
        if (tid >= 128 && tid < 136) { const int a = tid - 128; *(LAS v4u*)(sm + K_OFF + (a >> 1) * KS + 16 * (a & 1)) = (v4u){0u, 0u, 0u, 0u}; }
    }

    if (MODE != 2) {
        const int row = tid >> 3, oct = tid & 7;
        LAS char* p = sm + row * TROW + 16 * oct;
        *(LAS v2u*)(p + TU_OFF) = tu.xy; *(LAS v2u*)(p + TU_OFF + 8) = tu.zw;
        *(LAS v2u*)(p + TK_OFF + 3 * TROW) = tk.xy; *(LAS v2u*)(p + TK_OFF + 3 * TROW + 8) = tk.zw;
        *(LAS v2u*)(p + TV_OFF) = tv.xy; *(LAS v2u*)(p + TV_OFF + 8) = tv.zw;
        if (tid < 24) { *(LAS v2u*)(p + TK_OFF) = th.xy; *(LAS v2u*)(p + TK_OFF + 8) = th.zw; }
        STAMP(1);
        __syncthreads();
        const int cb = wv & 3, rp = wv >> 2, Jp = lane >> 4, q = (lane >> 2) & 3, pp = lane & 3, i = lane & 15;
        LAS char* tb = sm + (16 * Jp + 2 * rp + 4 * q) * TROW + 32 * cb + 8 * pp;
        v2u uc0 = trread(tb + TU_OFF), uc1 = trread(tb + TU_OFF + TROW);
        v2u kq[5];
#pragma unroll
        for (int z = 0; z < 5; ++z) kq[z] = trread(tb + TK_OFF + z * TROW);
        v2u vc0 = trread(tb + TV_OFF), vc1 = trread(tb + TV_OFF + TROW);
        const int so = ((gam * 32 + 8 * cb + (i >> 1)) * 32 + m) * 1024 + (2 * rp * 2 + (i & 1)) * 64 + Jp * 16;
        constexpr int AUX = MODE == 0 ? 16 : 0;
        __builtin_amdgcn_raw_buffer_store_b128((v4u){uc0.x, tag, uc0.y, tag}, wsr, so, 0, AUX);
        __builtin_amdgcn_raw_buffer_store_b128((v4u){uc1.x, tag, uc1.y, tag}, wsr, so + 128, 0, AUX);
        const float s0 = bflo(shv.x), s1 = bfhi(shv.x), s2 = bflo(shv.y), s3 = bfhi(shv.y);
        float kf[5][4];
#pragma unroll
        for (int z = 0; z < 5; ++z) { kf[z][0] = bflo(kq[z].x); kf[z][1] = bfhi(kq[z].x); kf[z][2] = bflo(kq[z].y); kf[z][3] = bfhi(kq[z].y); }
        float o0[4], o1[4];
        const float v0[4] = {bflo(vc0.x), bfhi(vc0.x), bflo(vc0.y), bfhi(vc0.y)};
        const float v1[4] = {bflo(vc1.x), bfhi(vc1.x), bflo(vc1.y), bfhi(vc1.y)};
#pragma unroll
        for (int e = 0; e < 4; ++e) {
            o0[e] = (((kf[0][e] * s3 + kf[1][e] * s2) + kf[2][e] * s1) + kf[3][e] * s0) * v0[e];
            o1[e] = (((kf[1][e] * s3 + kf[2][e] * s2) + kf[3][e] * s1) + kf[4][e] * s0) * v1[e];
        }
        __builtin_amdgcn_raw_buffer_store_b128((v4u){pkbf(o0[0], o0[1]), tag, pkbf(o0[2], o0[3]), tag}, wsr, so + 512, 0, AUX);
        __builtin_amdgcn_raw_buffer_store_b128((v4u){pkbf(o1[0], o1[1]), tag, pkbf(o1[2], o1[3]), tag}, wsr, so + 640, 0, AUX);
        STAMP(2);
        float acc = 0.f;
#pragma unroll
        for (int e = 0; e < 4; ++e) {
            const unsigned pr = pkbf(bflo(gu_[e]) * bflo(gw_[e]), bfhi(gu_[e]) * bfhi(gw_[e]));
            acc += bflo(pr) + bfhi(pr);
        }
#pragma unroll
        for (int s = 32; s >= 1; s >>= 1) acc += __shfl_xor(acc, s);
        const float lg = rbf(acc);
        const float g = rbf(1.f / (1.f + __expf(-lg)));
        if (lane == 0) __builtin_amdgcn_raw_buffer_store_b64((v2u){__float_as_uint(g), tag}, wsr, (int)WS_G + (64 * m + 8 * gam + wv) * 8, 0, AUX);
        STAMP(3);
    }
    if (MODE == 1) return;

    float kscale;
    {
        const float dt = rbf(expf(bflo(p_ldt)));
        const v2f ar = {rbf(expf(bflo(p_lar))), rbf(expf(bfhi(p_lar)))};
        const v2f zr = ar * (-dt) * 1.44269504088896341f;
        const v2f zi = (v2f){bflo(p_aim), bfhi(p_aim)} * dt * 0.15915494309189535f;
        unsigned mx = max(max(p_cre & 0x7fffu, (p_cre >> 16) & 0x7fffu), max(p_cim & 0x7fffu, (p_cim >> 16) & 0x7fffu));
#pragma unroll
        for (int s = 32; s >= 1; s >>= 1) mx = max(mx, (unsigned)__shfl_xor((int)mx, s));
        int ex = (int)(mx >> 7);
        ex = min(max(ex, 30), 230);
        const float cs = __uint_as_float((unsigned)(127 + 13 + 127 - ex) << 23);
        kscale = 2.f * dt * __uint_as_float((unsigned)(ex - 13 - 15) << 23);
        LAS char* fg = sm + FG_OFF + kc * FGC + 8 * lane;
        auto put = [&](int row, cplx2 c) { *(LAS v2u*)(fg + row * FGROW) = (v2u){pkh(c.re.x, c.im.x), pkh(c.re.y, c.im.y)}; };
        if (part < 2) {
            const cplx2 e8 = cexp_p(zr, zi, 8.f), e32 = cexp_p(zr, zi, 32.f);
            cplx2 b0 = cexp_p(zr, zi, part ? 128.f : 0.f);
            b0.re *= 32768.f; b0.im *= 32768.f;
            cplx2 c[4];
            c[0] = b0; c[1] = cmul(b0, e8); c[2] = cmul(c[1], e8); c[3] = cmul(c[2], e8);
#pragma unroll
            for (int a = 0; a < 4; ++a) {
#pragma unroll
                for (int b = 0; b < 4; ++b) { put(16 * part + 4 * a + b, c[b]); if (a < 3) c[b] = cmul(c[b], e32); }
            }
        } else {
            cplx2 C; C.re = (v2f){bflo(p_cre), bfhi(p_cre)} * cs; C.im = (v2f){bflo(p_cim), bfhi(p_cim)} * (-cs);
            const v2f zn = -zi;
            const cplx2 e256 = cexp_p(zr, zn, 256.f);
            if (part == 2) {
                const cplx2 e1 = cexp_p(zr, zn, 1.f), e2 = cexp_p(zr, zn, 2.f);
                cplx2 a = C, b = cmul(C, e1);
#pragma unroll
                for (int c = 0; c < 4; ++c) {
                    put(32 + 2 * c, a); put(32 + 2 * c + 1, b); put(32 + 8 + c, cmul(a, e256));
                    if (c < 3) { a = cmul(a, e2); b = cmul(b, e2); }
                }
            } else {
                const cplx2 e4 = cexp_p(zr, zn, 4.f), e512 = cexp_p(zr, zn, 512.f);
                cplx2 a = cmul(C, e512), b = cmul(a, e256);
                put(32 + 12, a); put(32 + 13, cmul(a, e4)); put(32 + 14, b); put(32 + 15, cmul(b, e4));
                a = cmul(a, e512); b = cmul(b, e512);
                put(32 + 16, a); put(32 + 17, b);
                a = cmul(a, e512); b = cmul(b, e512);
                put(32 + 18, a); put(32 + 19, b);
                put(32 + 20, cmul(a, e512));
            }
        }
    }
    *(LAS v4u*)(sm + K_OFF + (2 * (tid >> 8) + 1) * KS + 32 + 16 * (tid & 255)) = sk_;
    STAMP(4);
    __syncthreads();
    {
        const int rt = part >> 1, ct = part & 1;
        LAS char* ga = sm + FG_OFF + kc * FGC + (32 + 16 * rt + (lane & 15)) * FGROW + 16 * (lane >> 4);
        LAS char* fa = sm + FG_OFF + kc * FGC + (16 * ct + (lane & 15)) * FGROW + 16 * (lane >> 4);
        v4f acc = {0.f, 0.f, 0.f, 0.f};
#pragma unroll
        for (int ks = 0; ks < 8; ++ks)
            acc = __builtin_amdgcn_mfma_f32_16x16x32_f16(*(LAS v8h*)(ga + 64 * ks), *(LAS v8h*)(fa + 64 * ks), acc, 0, 0, 0);
        acc *= kscale;
        const int b = 16 * ct + (lane & 15), g = lane >> 4;
        LAS char* T = sm + T_OFF + kc * TS_;
        const unsigned w0 = pkbf(acc[0], acc[1]), w1 = pkbf(acc[2], acc[3]);
        if (rt == 0) {
            if (g < 2) *(LAS v2u*)(T + 2 * (8 * b + 4 * g)) = (v2u){w0, w1};
            else if (g == 2) *(LAS v2u*)(T + 2 * (256 + 4 * b)) = (v2u){w0, w1};
            else { *(LAS unsigned*)(T + 2 * (384 + 2 * b)) = w0; *(LAS unsigned*)(T + 2 * (448 + 2 * b)) = w1; }
        } else {
            if (g == 0) {
                *(LAS u16*)(T + 2 * (512 + b)) = (u16)w0; *(LAS u16*)(T + 2 * (544 + b)) = (u16)(w0 >> 16);
                *(LAS u16*)(T + 2 * (576 + b)) = (u16)w1; *(LAS u16*)(T + 2 * (608 + b)) = (u16)(w1 >> 16);
            } else if (g == 1 && b == 0) *(LAS u16*)(T + 2 * 640) = (u16)w0;
        }
    }
    __syncthreads();
    {
        const int ch = tid >> 8, l0 = 8 * (tid & 255);
        LAS char* T = sm + T_OFF + ch * TS_;
        unsigned e[8];
#pragma unroll
        for (int j = 0; j < 8; ++j) {
            const int l = l0 + j;
            const int tau = l < 256 ? l : (int)(pkbf((float)l, 0.f) & 0xffffu) - 17024;
            e[j] = *(LAS u16*)(T + 2 * tau);
        }
        *(LAS v4u*)(sm + K_OFF + (2 * ch) * KS + 32 + 2 * l0) = (v4u){e[0] | (e[1] << 16), e[2] | (e[3] << 16), e[4] | (e[5] << 16), e[6] | (e[7] << 16)};
    }
    STAMP(5);
    {
        constexpr int AUX = MODE == 0 ? 16 : 0;
        const int ib = (gam * 32 + m) * 32768 + 16 * lane;
        v4u x[4], gq[2];
        unsigned spins = 0;
        for (;;) {
            asm volatile("" ::: "memory");
#pragma unroll
            for (int j = 0; j < 4; ++j) x[j] = __builtin_amdgcn_raw_buffer_load_b128(wsr, ib + (8 * j + wv) * 1024, 0, AUX);
#pragma unroll
            for (int j = 0; j < 2; ++j) gq[j] = __builtin_amdgcn_raw_buffer_load_b128(wsr, (int)WS_G + tid * 32 + 16 * j, 0, AUX);
            if (MODE != 0) break;
            bool ok = true;
#pragma unroll
            for (int j = 0; j < 4; ++j) ok = ok && x[j].y == tag && x[j].w == tag;
#pragma unroll
            for (int j = 0; j < 2; ++j) ok = ok && gq[j].y == tag && gq[j].w == tag;
            if (__all(ok)) break;
            if (++spins > (1u << 16)) break;
        }
        const int a = lane >> 5, r = (lane >> 3) & 3, ch = (lane >> 2) & 1, Jp = lane & 3;
        LAS char* xp = sm + X_OFF + (2 * ch + a) * XC + r * XR + 128 + (4 * wv + Jp) * 8;
#pragma unroll
        for (int j = 0; j < 4; ++j) *(LAS v2u*)(xp + 256 * j) = (v2u){x[j].x, x[j].z};
        *(LAS v4u*)(sm + G_OFF + 16 * tid) = (v4u){gq[0].x, gq[0].z, gq[1].x, gq[1].z};
        if (tid < 32) *(LAS v4u*)(sm + Y_OFF + (tid >> 2) * YS + 16 * (tid & 3)) = (v4u){0u, 0u, 0u, 0u};
        if (tid >= 64 && tid < 72) *(LAS v4u*)(sm + C_OFF + (tid - 64) * CS) = (v4u){0u, 0u, 0u, 0u};
    }
    STAMP(6);
    __syncthreads();
    if (MODE == 0 && blockIdx.x == 0 && tid == 0) __builtin_amdgcn_raw_buffer_store_b32(tag, wsr, (int)WS_E, 0, 16);
    STAMP(7);

    {
        const int cc = wv & 3, h = wv >> 2;
        const int j = lane & 15, kb = lane >> 4, q = (lane >> 2) & 3, pp = lane & 3;
        LAS char* ka = sm + K_OFF + cc * KS + 32 + 32 * kb + 8 * (pp - q);
        LAS char* xa = sm + X_OFF + cc * XC + 2 * h * XR + 128 + 8 * (j - kb);
        const int Q = 4 * j + kb;
        LAS char* yw = sm + Y_OFF + (2 * cc + h) * YS + 64 + 16 * (Q ^ ((Q >> 3) & 3));
        LAS char* cw = sm + C_OFF + (2 * cc + h) * CS + 16 + 4 * Q;
        const int rho = 32 * wv + (lane >> 1), ech = lane & 1;
        const int Q0 = rho >> 2, Q1 = (rho - 2) >> 2;
        LAS char* y0 = sm + Y_OFF + (4 * ech) * YS + 64 + 16 * (Q0 ^ ((Q0 >> 3) & 3)) + 4 * (rho & 3);
        LAS char* y1 = sm + Y_OFF + (4 * ech + 1) * YS + 64 + 16 * (Q1 ^ ((Q1 >> 3) & 3)) + 4 * ((rho - 2) & 3);
        LAS char* cr = sm + C_OFF + (4 * ech + ((rho >> 1) & 1)) * CS + 16 + 4 * (Q0 - 1);
        LAS char* xe = sm + X_OFF + (2 * ech) * XC + (rho & 3) * XR + 128 + 2 * Q0;
        LAS char* ge = sm + G_OFF + 4 * rho;
        const bool usec = !(rho & 1);
        const float dsk = bflo(dskip[d0 + ech]), dss = bflo(ssmD[d0 + ech]);
        u16* op = out + (size_t)rho * 512 + d0 + ech;

        v4u A[16], B[2][16];
#pragma unroll
        for (int t = 0; t < 8; ++t) {
#pragma unroll
            for (int s = 2 * t; s < 2 * t + 2; ++s) {
                const v2u lo = trread(ka + 256 * s), hi = trread(ka + 256 * s + 128);
                A[s] = (v4u){lo.x, lo.y, hi.x, hi.y};
#pragma unroll
                for (int r = 0; r < 2; ++r) {
                    const v2u l2 = *(volatile LAS v2u*)(xa + r * XR + 64 * (s - 1)), h2 = *(volatile LAS v2u*)(xa + r * XR + 64 * (s - 1) - 32);
                    B[r][s] = (v4u){l2.x, l2.y, h2.x, h2.y};
                }
            }
            v4f a0 = {0.f, 0.f, 0.f, 0.f}, a1 = {0.f, 0.f, 0.f, 0.f};
#pragma unroll
            for (int s = 0; s < 2 * t + 2; ++s) {
                a0 = __builtin_amdgcn_mfma_f32_16x16x32_bf16(__builtin_bit_cast(v8bf, A[s]), __builtin_bit_cast(v8bf, B[0][2 * t + 1 - s]), a0, 0, 0, 0);
                a1 = __builtin_amdgcn_mfma_f32_16x16x32_bf16(__builtin_bit_cast(v8bf, A[s]), __builtin_bit_cast(v8bf, B[1][2 * t + 1 - s]), a1, 0, 0, 0);
            }
            *(LAS v4f*)(yw + 1024 * t) = (v4f){a0[0], a0[1] + a1[0], a0[2] + a1[1], a0[3] + a1[2]};
            *(LAS float*)(cw + 256 * t) = a1[3];
            __syncthreads();
            const float ca = *(LAS float*)(cr + 256 * t), cbv = *(LAS float*)(cr + 2 * CS + 256 * t);
            const float ya = *(LAS float*)(y0 + 1024 * t) + *(LAS float*)(y1 + 1024 * t) + (usec ? ca : 0.f);
            const float yb = *(LAS float*)(y0 + 2 * YS + 1024 * t) + *(LAS float*)(y1 + 2 * YS + 1024 * t) + (usec ? cbv : 0.f);
            const float uu = bflo(*(LAS u16*)(xe + 128 * t)), kvv = bflo(*(LAS u16*)(xe + XC + 128 * t));
            const float g = *(LAS float*)(ge + 1024 * t);
            const float a = ya + uu * dsk, b = uu * (yb + kvv * dss);
            op[(size_t)t * 256 * 512] = (u16)pkbf(b + g * (a - b), 0.f);
        }
    }
    STAMP(8);
}

extern "C" void kernel_launch(void* const* d_in, const int* in_sizes, int n_in, void* d_out, int out_size, void* d_ws,
                              size_t ws_size, hipStream_t stream) {
#define ARGS (const u16*)d_in[0], (const u16*)d_in[1], (const u16*)d_in[2], (const u16*)d_in[3], (const u16*)d_in[4], (const u16*)d_in[5], \
             (const u16*)d_in[6], (const u16*)d_in[8], (const u16*)d_in[9], (const u16*)d_in[10], (const u16*)d_in[11], (const u16*)d_in[12], \
             (const u16*)d_in[13], (u16*)d_out, (char*)d_ws
    (void)hipFuncSetAttribute((const void*)fused_kernel<0>, hipFuncAttributeMaxDynamicSharedMemorySize, LDS_BYTES);
    fused_kernel<0><<<256, 512, LDS_BYTES, stream>>>(ARGS);
}
```

```cpp
#include <hip/hip_runtime.h>
#include <stdint.h>

typedef unsigned short u16;
typedef unsigned v4u __attribute__((ext_vector_type(4)));
typedef unsigned v2u __attribute__((ext_vector_type(2)));
typedef short v4s __attribute__((ext_vector_type(4)));
typedef float v4f __attribute__((ext_vector_type(4)));
typedef float v2f __attribute__((ext_vector_type(2)));
typedef __bf16 v2bf __attribute__((ext_vector_type(2)));
typedef __bf16 v8bf __attribute__((ext_vector_type(8)));
typedef _Float16 v2h __attribute__((ext_vector_type(2)));
typedef _Float16 v8h __attribute__((ext_vector_type(8)));
#define LAS __attribute__((address_space(3)))
typedef __amdgpu_buffer_rsrc_t rsrc_t;

constexpr int XR = 1184;
constexpr int XC = 4 * XR + 8;
constexpr int X_OFF = 0;
constexpr int KS = 4096 + 32;
constexpr int K_OFF = 4 * XC;
constexpr int G_OFF = K_OFF + 4 * KS;
constexpr int S_OFF = G_OFF + 8192;
constexpr int TK1_OFF = S_OFF + 16;
constexpr int TR2 = 80, TW = 51 * TR2;
constexpr int U_OFF = TK1_OFF + 4 * KS;
constexpr int FG_OFF = U_OFF;
constexpr int FGR = 96;
constexpr int FGS = 64 * FGR + 64;
constexpr int FGC = 8 * FGS;
constexpr int FG_END = FG_OFF + 2 * FGC;
constexpr int YH = 8192 + 64, YCH = 2 * YH + 64, YC = 33280;
constexpr int Y_OFF = U_OFF;
constexpr int CP = 272, CT = 1280, CC = 9 * CT;
constexpr int C_OFF = Y_OFF + 2 * YC;
constexpr int LDS_BYTES = FG_END > C_OFF + 2 * CC ? FG_END : C_OFF + 2 * CC;
static_assert(LDS_BYTES <= 163840 && U_OFF % 16 == 0 && K_OFF % 16 == 0 && TW <= 4096 && CC % 256 == 0, "lds map");

constexpr int WS_G = 8 << 20;
constexpr int WS_E = WS_G + 8192;
constexpr int WS_DIAG = WS_E + 4096;
#define INBOX_AT 1
#define CODE_LINES 120

__device__ __forceinline__ float bflo(unsigned x) { return __uint_as_float(x << 16); }
__device__ __forceinline__ float bfhi(unsigned x) { return __uint_as_float(x & 0xffff0000u); }
__device__ __forceinline__ unsigned pkbf(float a, float b) { v2f t = {a, b}; return __builtin_bit_cast(unsigned, __builtin_convertvector(t, v2bf)); }
__device__ __forceinline__ unsigned pkh(v2f t) { return __builtin_bit_cast(unsigned, __builtin_convertvector(t, v2h)); }
__device__ __forceinline__ float rbf(float a) { return bflo(pkbf(a, a)); }
__device__ __forceinline__ float dot2(unsigned a, unsigned b, float c) {
    return __builtin_amdgcn_fdot2_f32_bf16(__builtin_bit_cast(v2bf, a), __builtin_bit_cast(v2bf, b), c, false);
}
__device__ __forceinline__ v2u trread(LAS char* p) { return __builtin_bit_cast(v2u, __builtin_amdgcn_ds_read_tr16_b64_v4i16((LAS v4s*)p)); }
template <int CTRL, int RM> __device__ __forceinline__ float dppf(float x) {
    return __int_as_float(__builtin_amdgcn_update_dpp(0, __float_as_int(x), CTRL, RM, 0xf, false));
}
template <int CTRL, int RM> __device__ __forceinline__ unsigned dppu(unsigned x) {
    return (unsigned)__builtin_amdgcn_update_dpp(0, (int)x, CTRL, RM, 0xf, false);
}
__device__ __forceinline__ float exp_acc(float x) {
    const float t = x * 1.44269502162933349609375f;
    const float lo = __builtin_fmaf(x, 1.92596299112661746e-8f, __builtin_fmaf(x, 1.44269502162933349609375f, -t));
    const float e = __builtin_amdgcn_exp2f(t);
    return __builtin_fmaf(e, lo * 0.693147182464599609375f, e);
}
struct cst { v2f b, p; };
__device__ __forceinline__ v2f cmul(v2f a, cst c) { return a.xx * c.b + a.yy * c.p; }
__device__ __forceinline__ cst cexp_p(float zr, float zi, float p) {
    const float e = __builtin_amdgcn_exp2f(zr * p), t = __builtin_amdgcn_fractf(zi * p);
    const float c = e * __builtin_amdgcn_cosf(t), s = e * __builtin_amdgcn_sinf(t);
    cst r; r.b = (v2f){c, s}; r.p = (v2f){-s, c};
    return r;
}
#define SB() __builtin_amdgcn_sched_barrier(0)
#define BAR() do { asm volatile("s_waitcnt lgkmcnt(0)\n\ts_barrier" ::: "memory"); } while (0)
#define PIN(x) asm volatile("" : "+v"(x))

#define NSTAMP 24
#define STAMP(i)
#define NRETRY
#define BARS(i) BAR()

struct Args {
    const u16 *u, *lar, *aim, *cre, *cim, *ldt, *dskip, *kk, *vv, *shk, *ssmk, *ssmD, *gw;
    u16* out;
    char* ws;
};
struct Tile { v4u u, k, v, h; };

template <bool ISP>
__device__ __forceinline__ void body(const Args& a, LAS char* sm, const int tid, const int wv, const char* pc0) {
    const int lane = tid & 63;
    const int gam = blockIdx.x & 7, m = blockIdx.x >> 3;
    const int d0 = 64 * gam + 2 * m;
    const rsrc_t wsr = __builtin_amdgcn_make_buffer_rsrc(a.ws, 0, WS_E + 64, 0x00020000);
    const rsrc_t kr = __builtin_amdgcn_make_buffer_rsrc((void*)a.kk, 0, 2048 * 1024, 0x00020000);
    const int Jp = wv & 1, chalf = (wv >> 1) & 1;
    const int J = (ISP ? 0 : 64) + 2 * m + Jp;
    const int toff = (16 * J + (lane >> 2)) * 1024 + 128 * gam + 64 * chalf + 16 * (lane & 3);
    Tile t;
    unsigned tag;
    v4u ga0, ga1, gw0, gw1, sk0, sk1;
    unsigned p_lar, p_aim, p_cre, p_cim, p_ldt, m_cre, m_cim;
#define TILE_LOAD() do { t.u = *(const v4u*)((const char*)a.u + toff); t.k = __builtin_amdgcn_raw_buffer_load_b128(kr, toff, 0, 0); \
        t.h = __builtin_amdgcn_raw_buffer_load_b128(kr, lane < 12 ? toff - 3 * 1024 : -16, 0, 0); t.v = *(const v4u*)((const char*)a.vv + toff); } while (0)
    if (ISP) {
        TILE_LOAD();
        tag = __builtin_amdgcn_raw_buffer_load_b32(wsr, WS_E, 0, 16);
    } else {
        const int pc = d0 + chalf, n = 64 * Jp + lane;
        p_ldt = a.ldt[pc];
        p_lar = a.lar[pc * 128 + n]; p_aim = a.aim[pc * 128 + n]; p_cre = a.cre[pc * 128 + n]; p_cim = a.cim[pc * 128 + n];
        m_cre = ((const unsigned*)a.cre)[pc * 64 + lane]; m_cim = ((const unsigned*)a.cim)[pc * 64 + lane];
        unsigned cw = *(const __attribute__((address_space(1))) unsigned*)(size_t)(pc0 + 128 * min(64 * (m & 3) + 16 * (wv & 3) + (lane & 15), CODE_LINES - 1));
        tag = cw;
    }
    const v2u shv = *(const v2u*)(a.shk + (64 * gam + 32 * chalf + 16 * (lane >> 5) + (lane & 15)) * 4);
    if (ISP) {
        const char* grow = (const char*)a.u + (1024 * (lane >> 5) + 4 * (int)blockIdx.x + wv) * 1024 + 16 * (lane & 31);
        ga0 = *(const v4u*)grow; ga1 = *(const v4u*)(grow + 512);
        gw0 = *(const v4u*)((const char*)a.gw + 16 * (lane & 31)); gw1 = *(const v4u*)((const char*)a.gw + 512 + 16 * (lane & 31));
    }
    const unsigned dsk_ = a.dskip[d0 + (lane & 1)], dss_ = a.ssmD[d0 + (lane & 1)];
    SB();
    LAS char* const tw = sm + (ISP ? K_OFF + 32 : TK1_OFF) + (wv & 3) * KS;
    const int q = (lane >> 2) & 3, pp = lane & 3, i16 = lane & 15, rp = (lane >> 4) & 1, hi = lane >> 5, kb = lane >> 4;
    LAS char* tp = tw + (lane >> 2) * TR2 + 16 * pp;
    LAS char* tb = tw + (2 * rp + 4 * q) * TR2 + 32 * hi + 8 * pp;
    LAS char* tu = tb + hi * (TR2 - 32);
    LAS char* tk = tb - 3 * q * TR2;
    int so = ((gam * 32 + 16 * chalf + (i16 >> 1)) * 128 + J) * 256 + (2 * (2 * rp + hi) + (i16 & 1)) * 16;
    int so2 = so + 8 * 32768;
    LAS char* fa = sm + FG_OFF + chalf * FGC + i16 * FGR + 16 * kb;
    LAS char* kw = sm + K_OFF + 2 * chalf * KS + 32 + 16 * (16 * Jp + i16) + (ISP ? 512 * hi + 8 * rp : 1024 + 512 * (kb + (kb == 3)));
    int ib = (gam * 32 + m) * 32768 + 16 * tid;
    LAS char* xp = sm + X_OFF + (2 * (tid & 1) + ((tid >> 3) & 1)) * XC + ((tid >> 1) & 3) * XR + 128 + 8 * (tid >> 4);
    const int cc = wv & 3, h = ISP ? 0 : 1;
    LAS char* ka = sm + K_OFF + cc * KS + 32 + 32 * kb + 8 * (pp - q);
    PIN(tp); PIN(tu); PIN(tb); PIN(tk); PIN(so); PIN(so2); PIN(fa); PIN(kw); PIN(ib); PIN(xp); PIN(ka);
    LAS char *xa, *yw, *cw, *y0, *y1, *cr, *xe, *ge;
    int oo;
    auto conv_addr = [&]() {
        xa = sm + X_OFF + cc * XC + 2 * h * XR + 128 - 96 + 8 * (i16 - kb);
        const int Q = 4 * i16 + kb;
        yw = sm + Y_OFF + (cc & 1) * YC + (cc >> 1) * YCH + h * YH + 64 + 16 * (Q ^ ((Q >> 3) & 3));
        cw = sm + C_OFF + (cc & 1) * CC + ((cc & 2) + h) * CP + (Q < 63 ? 4 * (Q + 1) : CT);
        const int rho = tid >> 1, ech = lane & 1;
        const int Q0 = rho >> 2, Q1 = (rho - 2) >> 2;
        y0 = sm + Y_OFF + ech * YCH + 64 + 16 * (Q0 ^ cc) + 4 * (rho & 3);
        y1 = sm + Y_OFF + ech * YCH + YH + 64 + 16 * (Q1 ^ ((Q1 >> 3) & 3)) + 4 * ((rho - 2) & 3);
        cr = sm + C_OFF + ((rho & 1) ? 1152 : (2 * ech + ((rho >> 1) & 1)) * CP + 4 * Q0);
        xe = sm + X_OFF + (2 * ech) * XC + (rho & 3) * XR + 128 + 2 * Q0;
        ge = sm + G_OFF + 4 * rho;
        oo = rho * 1024 + 2 * (d0 + ech);
        PIN(xa); PIN(yw); PIN(cw); PIN(y0); PIN(y1); PIN(cr); PIN(xe); PIN(ge); PIN(oo);
    };
    if (ISP) {
        *(LAS v2u*)(sm + X_OFF + wv * XC + kb * XR + 8 * i16) = (v2u){0u, 0u};
        *(LAS v2u*)(sm + K_OFF + wv * KS + 8 * pp) = (v2u){0u, 0u};
    } else conv_addr();
    SB();

    auto produce = [&]() {
        *(LAS v4u*)(tp) = t.u;
        *(LAS v4u*)(tp + 35 * TR2) = t.k;
        if (lane < 12) *(LAS v4u*)(tp + 32 * TR2) = t.h;
        *(LAS v4u*)(tp + 16 * TR2) = t.v;
        asm volatile("" ::: "memory");
        const v2u uc0 = trread(tu), uc1 = trread(tu + 32);
        v2u kq[2][4];
#pragma unroll
        for (int e = 0; e < 4; ++e) { kq[0][e] = trread(tk + (32 + 4 * e) * TR2); kq[1][e] = trread(tk + (33 + 4 * e) * TR2); }
        const v2u vc0 = trread(tb + 16 * TR2), vc1 = trread(tb + 17 * TR2);
        SB();
        const unsigned s32 = __builtin_amdgcn_alignbit(shv.y, shv.y, 16), s10 = __builtin_amdgcn_alignbit(shv.x, shv.x, 16);
        __builtin_amdgcn_raw_buffer_store_b128((v4u){uc0.x, tag, uc0.y, tag}, wsr, so, 0, 16);
        __builtin_amdgcn_raw_buffer_store_b128((v4u){uc1.x, tag, uc1.y, tag}, wsr, so2, 0, 16);
        float o0[4], o1[4];
#pragma unroll
        for (int e = 0; e < 4; ++e) {
            o0[e] = dot2(kq[0][e].y, s10, dot2(kq[0][e].x, s32, 0.f));
            o1[e] = dot2(kq[1][e].y, s10, dot2(kq[1][e].x, s32, 0.f));
        }
        const auto w0 = __builtin_amdgcn_permlane32_swap(pkbf(o0[0] * bflo(vc0.x), o0[1] * bfhi(vc0.x)), pkbf(o1[0] * bflo(vc1.x), o1[1] * bfhi(vc1.x)), false, false);
        const auto w1 = __builtin_amdgcn_permlane32_swap(pkbf(o0[2] * bflo(vc0.y), o0[3] * bfhi(vc0.y)), pkbf(o1[2] * bflo(vc1.y), o1[3] * bfhi(vc1.y)), false, false);
        __builtin_amdgcn_raw_buffer_store_b128((v4u){w0[0], tag, w1[0], tag}, wsr, so + 128, 0, 16);
        __builtin_amdgcn_raw_buffer_store_b128((v4u){w0[1], tag, w1[1], tag}, wsr, so2 + 128, 0, 16);
        SB();
    };

    if (ISP) {
        __builtin_amdgcn_s_setprio(1);
        STAMP(1);
        tag = tag * 0x9E3779B1u + 0x7F4A7C15u;
        produce();
        STAMP(2);
        *(LAS unsigned*)(sm + S_OFF + 8) = tag;
        float acc = 0.f, acc2 = 0.f;
#pragma unroll
        for (int e = 0; e < 4; ++e) { acc = dot2(ga0[e], gw0[e], acc); acc2 = dot2(ga1[e], gw1[e], acc2); }
        acc += acc2;
        acc += dppf<0xB1, 0xf>(acc); acc += dppf<0x4E, 0xf>(acc); acc += dppf<0x141, 0xf>(acc); acc += dppf<0x140, 0xf>(acc);
        acc += dppf<0x142, 0xa>(acc);
        const float g = __builtin_amdgcn_rcpf(1.f + __builtin_amdgcn_exp2f(rbf(acc) * -1.44269504088896341f));
        const float g0 = __int_as_float(__builtin_amdgcn_readlane(__float_as_int(g), 31)), g1 = __int_as_float(__builtin_amdgcn_readlane(__float_as_int(g), 63));
        __builtin_amdgcn_raw_buffer_store_b64((v2u){pkbf(g0, g1), tag}, wsr, lane == 0 ? WS_G + (4 * (int)blockIdx.x + wv) * 8 : -16, 0, 16);
        STAMP(3);
    } else {
        const float dt = rbf(exp_acc(bflo(p_ldt)));
        SB();
        STAMP(1);
        const char* skp = (const char*)a.ssmk + (d0 + chalf) * 4096 + 16 * (tid & 127);
        sk0 = *(const v4u*)skp;
        sk1 = *(const v4u*)(skp + 2048);
        SB();
        TILE_LOAD();
        SB();
        const float zr = rbf(exp_acc(bflo(p_lar))) * (-dt) * 1.44269504088896341f;
        const float zi = bflo(p_aim) * dt * 0.15915494309189535f;
        const cst e8 = cexp_p(zr, zi, 8.f), e64 = cexp_p(zr, zi, 64.f), e1 = cexp_p(zr, -zi, 1.f), e256 = cexp_p(zr, -zi, 256.f);
        unsigned mx = max(max(m_cre & 0x7fffu, (m_cre >> 16) & 0x7fffu), max(m_cim & 0x7fffu, (m_cim >> 16) & 0x7fffu));
        mx = max(mx, dppu<0xB1, 0xf>(mx)); mx = max(mx, dppu<0x4E, 0xf>(mx)); mx = max(mx, dppu<0x141, 0xf>(mx)); mx = max(mx, dppu<0x140, 0xf>(mx));
        mx = max(mx, dppu<0x142, 0xa>(mx)); mx = max(mx, dppu<0x143, 0xc>(mx));
        int ex = __builtin_amdgcn_readlane((int)mx, 63) >> 7;
        ex = min(max(ex, 30), 230);
        const float cs = __uint_as_float((unsigned)(127 + 13 + 127 - ex) << 23);
        *(LAS float*)(sm + S_OFF + 4 * chalf) = 2.f * dt * __uint_as_float((unsigned)(ex - 13 - 15) << 23);
        LAS char* fg = sm + FG_OFF + chalf * FGC + (4 * Jp + kb) * FGS + 4 * i16;
#define PUT(row, c) *(LAS unsigned*)(fg + (row) * FGR) = pkh(c)
        v2f f[8], g[9];
        f[0] = (v2f){32768.f, 0.f}; f[1] = e8.b * 32768.f;
        g[0] = (v2f){bflo(p_cre) * cs, bflo(p_cim) * -cs};
#pragma unroll
        for (int b = 1; b < 9; ++b) { if (b > 1 && b < 8) f[b] = cmul(f[b - 1], e8); g[b] = cmul(g[b - 1], e1); }
#pragma unroll
        for (int b = 0; b < 8; ++b) { PUT(b, f[b]); }
#pragma unroll
        for (int b = 0; b < 8; ++b) { PUT(32 + b, g[b]); }
        v2f gA = cmul(g[0], e256), gB = cmul(g[4], e256), gC = cmul(g[8], e256);
        PUT(40, gA); PUT(41, cmul(g[2], e256)); PUT(42, gB); PUT(44, gB); PUT(45, cmul(g[6], e256)); PUT(46, gC);
        f[0] = e64.b * 32768.f;
#pragma unroll
        for (int z = 1; z < 4; ++z) {
#pragma unroll
            for (int b = 0; b < 8; ++b) { if (z > 1 || b > 0) f[b] = cmul(f[b], e64); PUT(8 * z + b, f[b]); }
            if (z < 3) { gA = cmul(gA, e256); gB = cmul(gB, e256); gC = cmul(gC, e256); PUT(44 + 4 * z, gA); PUT(45 + 4 * z, gB); PUT(46 + 4 * z, gC); }
        }
#pragma unroll
        for (int z = 0; z < 4; ++z) { gA = cmul(gA, e256); gC = cmul(gC, e256); PUT(56 + 2 * z, gA); PUT(57 + 2 * z, gC); }
        STAMP(2);
        STAMP(3);
    }
    BAR();
    STAMP(4);
    v4u x0, x1, gq;
#define INBOX(sg) do { x0 = __builtin_amdgcn_raw_buffer_load_b128(wsr, ib, (sg) * 16384, 16); x1 = __builtin_amdgcn_raw_buffer_load_b128(wsr, ib, (sg) * 16384 + 8192, 16); \
        if ((sg) == 0) gq = __builtin_amdgcn_raw_buffer_load_b128(wsr, 16 * tid, WS_G, 16); } while (0)
#define FRESH(sg) __all(x0.y == tag && x0.w == tag && x1.y == tag && x1.w == tag && ((sg) || (gq.y == tag && gq.w == tag)))
#define TAKE(sg) do { \
        if (__builtin_expect(!FRESH(sg), 0)) { unsigned spins = 0; do { asm volatile("" ::: "memory"); INBOX(sg); NRETRY; } while (!FRESH(sg) && ++spins < (1u << 16)); } \
        *(LAS v2u*)(xp + 512 * (sg)) = (v2u){x0.x, x0.z}; *(LAS v2u*)(xp + 512 * (sg) + 256) = (v2u){x1.x, x1.z}; \
        if ((sg) == 0) { *(LAS v2u*)(sm + G_OFF + 8 * tid) = (v2u){gq.x << 16, gq.z << 16}; *(LAS v2u*)(sm + G_OFF + 4096 + 8 * tid) = (v2u){gq.x & 0xffff0000u, gq.z & 0xffff0000u}; } } while (0)
    {
        v8h fa_[8], ga_[8];
#pragma unroll
        for (int ks = 0; ks < 8; ++ks) { ga_[ks] = *(LAS v8h*)(fa + FGS * ks + (ISP ? 32 : 48) * FGR); fa_[ks] = *(LAS v8h*)(fa + FGS * ks + 16 * Jp * FGR); }
        const float kscale = *(LAS float*)(sm + S_OFF + 4 * chalf);
        if (!ISP) { asm volatile("" :: "v"(tag)); tag = *(LAS unsigned*)(sm + S_OFF + 8); }
        SB();
        unsigned sa, sb, sc;
        if (ISP) { sa = hi ? 0x01000100u : 0x03020100u; sb = hi ? 0x05040302u : 0x07060504u; sc = 0; }
        else { sa = hi ? 0x01000100u : 0x03020100u; sb = (hi && !(i16 & 1)) ? 0x03020100u : 0x03020302u; sc = hi ? 0x03020302u : 0x05040504u; }
        PIN(sa); PIN(sb); PIN(sc);
        SB();
        v4f acc = {0.f, 0.f, 0.f, 0.f};
#pragma unroll
        for (int ks = 0; ks < 8; ++ks) acc = __builtin_amdgcn_mfma_f32_16x16x32_f16(ga_[ks], fa_[ks], acc, 0, 0, 0);
        acc *= kscale;
        const unsigned w0 = pkbf(acc[0], acc[1]), w1 = pkbf(acc[2], acc[3]);
        if (ISP) {
            *(LAS v2u*)kw = (v2u){__builtin_amdgcn_perm(w1, w0, sa), __builtin_amdgcn_perm(w1, w0, sb)};
        } else {
            *(LAS v4u*)kw = (v4u){__builtin_amdgcn_perm(w1, w0, 0x01000100u), __builtin_amdgcn_perm(w1, w0, sa), __builtin_amdgcn_perm(w1, w0, sb), __builtin_amdgcn_perm(w1, w0, sc)};
            const unsigned d22 = __builtin_amdgcn_perm(w1, w1, 0x01000100u);
            if (hi) *(LAS v4u*)(kw + 512) = (v4u){d22, d22, __builtin_amdgcn_perm(w1, w1, sb), __builtin_amdgcn_perm(w1, w1, 0x03020302u)};
            LAS char* skw = sm + K_OFF + (2 * chalf + 1) * KS + 32 + 16 * (tid & 127);
            *(LAS v4u*)skw = sk0; *(LAS v4u*)(skw + 2048) = sk1;
        }
    }
    BAR();
    STAMP(5);
    INBOX(0);
    SB();
    STAMP(6);
    v2u A[16][2], B[2][16][2];
#define LA(s) do { A[s][0] = trread(ka + 256 * (s)); A[s][1] = trread(ka + 256 * (s) + 128); } while (0)
    if (ISP) {
        if (tid < 32) *(LAS v4u*)(sm + Y_OFF + (tid >> 4) * YC + ((tid >> 3) & 1) * YCH + ((tid >> 2) & 1) * YH + 16 * (tid & 3)) = (v4u){0u, 0u, 0u, 0u};
        else if (tid < 48) *(LAS unsigned*)(sm + C_OFF + ((tid >> 3) & 1) * CC + (tid & 7) * CT + 1152) = 0u;
        else if (tid < 56) *(LAS unsigned*)(sm + C_OFF + ((tid >> 2) & 1) * CC + (tid & 3) * CP) = 0u;
        __builtin_amdgcn_s_setprio(0);
        conv_addr();
    } else {
        __builtin_amdgcn_s_setprio(1);
        produce();
    }
#pragma unroll
    for (int s = 0; s < 16; ++s) LA(s);
    SB();
    STAMP(7);
    TAKE(0);
    STAMP(8);
    const float dsk = bflo(dsk_), dss = bflo(dss_);
    const rsrc_t outr = __builtin_amdgcn_make_buffer_rsrc(a.out, 0, 2048 * 1024, 0x00020000);
    BAR();
    STAMP(9);
    v4f acc[8][2], zv[8];
    float z1[8], z2[8], e_a0[8], e_a1[8], e_ac[8], e_b0[8], e_b1[8], e_bc[8], e_g[8];
    unsigned e_u[8], e_kv[8];
#define LB(idx, r, hf) B[r][idx][hf] = *(volatile LAS v2u*)(xa + (r) * XR + 64 * (idx) + 32 - 32 * (hf))
#define FRAG(F_) __builtin_bit_cast(v8bf, (v4u){F_[0].x, F_[0].y, F_[1].x, F_[1].y})
#define MF(t, r, s, idx) acc[t][r] = __builtin_amdgcn_mfma_f32_16x16x32_bf16(FRAG(A[s]), FRAG(B[r][idx]), acc[t][r], 0, 0, 0)
#define MF0(t, r, s, idx) acc[t][r] = __builtin_amdgcn_mfma_f32_16x16x32_bf16(FRAG(A[s]), FRAG(B[r][idx]), (v4f){0.f, 0.f, 0.f, 0.f}, 0, 0, 0)
#define ZA(t) do { z1[t] = acc[t][0][1] + acc[t][1][0]; PIN(z1[t]); z2[t] = acc[t][0][2] + acc[t][1][1]; PIN(z2[t]); } while (0)
#define ZB(t) do { float z3 = acc[t][0][3] + acc[t][1][2]; PIN(z3); zv[t] = (v4f){acc[t][0][0], z1[t], z2[t], z3}; *(LAS v4f*)(yw + 1024 * (t)) = zv[t]; } while (0)
#define ZC(t) do { *(LAS float*)(cw + CT * (t)) = acc[t][1][3]; } while (0)
#define ZD(t) asm volatile("" :: "v"(zv[t]), "v"(acc[t][1]))
#define PRIO(x) __builtin_amdgcn_s_setprio(x)
#define ER(t) do { e_a0[t] = *(LAS float*)(y0 + 1024 * (t)); e_b0[t] = *(LAS float*)(y0 + 1024 * (t) + YC); e_a1[t] = *(LAS float*)(y1 + 1024 * (t)); e_b1[t] = *(LAS float*)(y1 + 1024 * (t) + YC); \
        e_ac[t] = *(LAS float*)(cr + CT * (t)); e_bc[t] = *(LAS float*)(cr + CT * (t) + CC); \
        e_u[t] = *(LAS u16*)(xe + 128 * (t)); e_kv[t] = *(LAS u16*)(xe + XC + 128 * (t)); e_g[t] = *(LAS float*)(ge + 1024 * (t)); } while (0)
#define EV(t) do { const float uu = bflo(e_u[t]); const float ya = (e_a0[t] + e_a1[t]) + e_ac[t] + uu * dsk; const float yb = ((e_b0[t] + e_b1[t]) + e_bc[t] + bflo(e_kv[t]) * dss) * uu; \
        __builtin_amdgcn_raw_buffer_store_b16((u16)pkbf(yb + e_g[t] * (ya - yb), 0.f), outr, oo, (t) * 262144, 0); } while (0)
#define ISSUE1() INBOX(1)
#define CONSUME1() TAKE(1)
    if (ISP) {
        LB(0,0,0); LB(0,0,1); LB(0,1,0); LB(0,1,1); LB(1,0,0); LB(1,0,1); LB(1,1,0); LB(1,1,1); LB(2,0,0); LB(2,0,1); LB(2,1,0); LB(2,1,1); LB(3,0,0); LB(3,0,1); LB(3,1,0); LB(3,1,1); SB();
        PRIO(1); SB();
        MF0(0,0,1,0); SB(); LB(4,0,0); LB(4,0,1); LB(4,1,0); LB(4,1,1); SB();
        MF0(0,1,1,0); SB(); LB(5,0,0); LB(5,0,1); LB(5,1,0); LB(5,1,1); SB();
        MF(0,0,0,1); SB(); LB(6,0,0); LB(6,0,1); LB(6,1,0); LB(6,1,1); SB();
        MF(0,1,0,1); SB(); LB(7,0,0); LB(7,0,1); LB(7,1,0); LB(7,1,1); SB();
        MF0(1,0,2,1); SB();
        MF0(1,1,2,1); SB();
        MF(1,0,3,0); SB(); ZA(0); SB();
        MF(1,1,3,0); SB(); ZB(0); SB();
        MF(1,0,1,2); SB(); ZC(0); SB();
        MF(1,1,1,2); SB();  SB();
        MF(1,0,0,3); SB(); ZD(0); SB();
        MF(1,1,0,3); SB();
        MF0(2,0,2,3); SB();
        MF0(2,1,2,3); SB();
        MF(2,0,3,2); SB(); ZA(1); SB();
        MF(2,1,3,2); SB(); ZB(1); SB();
        MF(2,0,4,1); SB(); ZC(1); SB();
        MF(2,1,4,1); SB();  SB();
        MF(2,0,5,0); SB(); ZD(1); SB();
        MF(2,1,5,0); SB();
        PRIO(2); SB();
        BARS(0); SB();
        ISSUE1(); SB();
        PRIO(1); SB();
        MF(2,0,1,4); SB();
        MF(2,1,1,4); SB();
        MF(2,0,0,5); SB();
        MF(2,1,0,5); SB();
        MF0(3,0,2,5); SB();
        MF0(3,1,2,5); SB();
        MF(3,0,3,4); SB(); ZA(2); SB();
        MF(3,1,3,4); SB(); ZB(2); SB();
        MF(3,0,4,3); SB(); ZC(2); SB();
        MF(3,1,4,3); SB();  SB();
        MF(3,0,5,2); SB(); ZD(2); SB();
        MF(3,1,5,2); SB();
        MF(3,0,6,1); SB();
        MF(3,1,6,1); SB();
        MF(3,0,7,0); SB();
        MF(3,1,7,0); SB();
        MF(3,0,1,6); SB();
        MF(3,1,1,6); SB();
        MF(3,0,0,7); SB();
        MF(3,1,0,7); SB();
        PRIO(2); SB();
        ER(0); SB();
        ER(1); SB();
        EV(0); SB();
        EV(1); SB();
        BARS(1); SB();
        PRIO(1); SB();
        MF0(4,0,2,7); SB();
        MF0(4,1,2,7); SB();
        MF(4,0,3,6); SB(); ZA(3); SB();
        MF(4,1,3,6); SB(); ZB(3); SB();
        MF(4,0,4,5); SB(); ZC(3); SB();
        MF(4,1,4,5); SB();  SB();
        MF(4,0,5,4); SB(); ZD(3); SB();
        MF(4,1,5,4); SB();
        MF(4,0,6,3); SB();
        MF(4,1,6,3); SB();
        MF(4,0,7,2); SB();
        MF(4,1,7,2); SB();
        MF(4,0,8,1); SB();
        MF(4,1,8,1); SB();
        MF(4,0,9,0); SB();
        MF(4,1,9,0); SB();
        MF0(5,0,4,7); SB();
        MF0(5,1,4,7); SB();
        MF(5,0,5,6); SB();
        MF(5,1,5,6); SB();
        MF(5,0,6,5); SB();
        MF(5,1,6,5); SB();
        PRIO(2); SB();
        ER(2); SB();
        EV(2); SB();
        CONSUME1(); SB();
        BARS(2); SB();
        PRIO(1); SB();
        MF(5,0,7,4); SB(); LB(8,0,0); LB(8,0,1); SB();
        MF(5,1,7,4); SB(); LB(8,1,0); LB(8,1,1); SB();
        MF(5,0,8,3); SB(); LB(9,0,0); LB(9,0,1); SB();
        MF(5,1,8,3); SB(); LB(9,1,0); LB(9,1,1); SB();
        MF(5,0,9,2); SB(); LB(10,0,0); LB(10,0,1); SB();
        MF(5,1,9,2); SB(); LB(10,1,0); LB(10,1,1); SB();
        MF(5,0,10,1); SB(); LB(11,0,0); LB(11,0,1); SB();
        MF(5,1,10,1); SB(); LB(11,1,0); LB(11,1,1); SB();
        MF(5,0,11,0); SB();
        MF(5,1,11,0); SB();
        MF(4,0,1,8); SB();
        MF(4,1,1,8); SB();
        MF(4,0,0,9); SB();
        MF(4,1,0,9); SB();
        MF(5,0,3,8); SB();
        MF(5,1,3,8); SB();
        MF(5,0,2,9); SB(); ZA(4); SB();
        MF(5,1,2,9); SB(); ZB(4); SB();
        MF(5,0,1,10); SB(); ZC(4); SB();
        MF(5,1,1,10); SB();  SB();
        MF(5,0,0,11); SB(); ZD(4); SB();
        MF(5,1,0,11); SB();
        MF0(6,0,2,11); SB();
        MF0(6,1,2,11); SB();
        MF(6,0,3,10); SB(); ZA(5); SB();
        MF(6,1,3,10); SB(); ZB(5); SB();
        MF(6,0,4,9); SB(); ZC(5); SB();
        MF(6,1,4,9); SB();  SB();
        MF(6,0,5,8); SB(); ZD(5); SB();
        MF(6,1,5,8); SB();
        PRIO(2); SB();
        ER(3); SB();
        EV(3); SB();
        BARS(3); SB();
        PRIO(1); SB();
        MF(6,0,6,7); SB(); LB(12,0,0); LB(12,0,1); SB();
        MF(6,1,6,7); SB(); LB(12,1,0); LB(12,1,1); SB();
        MF(6,0,7,6); SB(); LB(13,0,0); LB(13,0,1); SB();
        MF(6,1,7,6); SB(); LB(13,1,0); LB(13,1,1); SB();
        MF(6,0,8,5); SB();
        MF(6,1,8,5); SB();
        MF(6,0,9,4); SB();
        MF(6,1,9,4); SB();
        MF(6,0,10,3); SB();
        MF(6,1,10,3); SB();
        MF(6,0,11,2); SB();
        MF(6,1,11,2); SB();
        MF(6,0,12,1); SB();
        MF(6,1,12,1); SB();
        MF(6,0,13,0); SB();
        MF(6,1,13,0); SB();
        MF(6,0,1,12); SB();
        MF(6,1,1,12); SB();
        MF(6,0,0,13); SB();
        MF(6,1,0,13); SB();
        MF0(7,0,2,13); SB();
        MF0(7,1,2,13); SB();
        MF(7,0,3,12); SB(); ZA(6); SB();
        MF(7,1,3,12); SB(); ZB(6); SB();
        MF(7,0,4,11); SB(); ZC(6); SB();
        MF(7,1,4,11); SB();  SB();
        MF(7,0,5,10); SB(); ZD(6); SB();
        MF(7,1,5,10); SB();
        MF(7,0,6,9); SB();
        MF(7,1,6,9); SB();
        PRIO(2); SB();
        ER(4); SB();
        ER(5); SB();
        EV(4); SB();
        EV(5); SB();
        BARS(4); SB();
        PRIO(1); SB();
        MF(7,0,7,8); SB(); LB(14,0,0); LB(14,0,1); SB();
        MF(7,1,7,8); SB(); LB(14,1,0); LB(14,1,1); SB();
        MF(7,0,8,7); SB(); LB(15,0,0); LB(15,0,1); SB();
        MF(7,1,8,7); SB(); LB(15,1,0); LB(15,1,1); SB();
        MF(7,0,9,6); SB();
        MF(7,1,9,6); SB();
        MF(7,0,10,5); SB();
        MF(7,1,10,5); SB();
        MF(7,0,11,4); SB();
        MF(7,1,11,4); SB();
        MF(7,0,12,3); SB();
        MF(7,1,12,3); SB();
        MF(7,0,13,2); SB();
        MF(7,1,13,2); SB();
        MF(7,0,14,1); SB();
        MF(7,1,14,1); SB();
        MF(7,0,15,0); SB();
        MF(7,1,15,0); SB();
        MF(7,0,1,14); SB();
        MF(7,1,1,14); SB();
        MF(7,0,0,15); SB();
        MF(7,1,0,15); SB();
        ZA(7); ZB(7); ZC(7);  ZD(7); SB();
        PRIO(2); SB();
        ER(6); SB();
        EV(6); SB();
        BARS(5); SB();
        ER(7); EV(7);
    } else {
        LB(0,0,0); LB(0,0,1); LB(0,1,0); LB(0,1,1); LB(1,0,0); LB(1,0,1); LB(1,1,0); LB(1,1,1); LB(2,0,0); LB(2,0,1); LB(2,1,0); LB(2,1,1); LB(3,0,0); LB(3,0,1); LB(3,1,0); LB(3,1,1); SB();
        PRIO(0); SB();
        MF0(0,0,1,0); SB(); LB(4,0,0); LB(4,0,1); LB(4,1,0); LB(4,1,1); SB();
        MF0(0,1,1,0); SB(); LB(5,0,0); LB(5,0,1); LB(5,1,0); LB(5,1,1); SB();
        MF(0,0,0,1); SB(); LB(6,0,0); LB(6,0,1); LB(6,1,0); LB(6,1,1); SB();
        MF(0,1,0,1); SB(); LB(7,0,0); LB(7,0,1); LB(7,1,0); LB(7,1,1); SB();
        MF0(1,0,2,1); SB();
        MF0(1,1,2,1); SB();
        MF(1,0,3,0); SB(); ZA(0); SB();
        MF(1,1,3,0); SB(); ZB(0); SB();
        MF(1,0,1,2); SB(); ZC(0); SB();
        MF(1,1,1,2); SB();  SB();
        MF(1,0,0,3); SB(); ZD(0); SB();
        MF(1,1,0,3); SB();
        MF0(2,0,2,3); SB();
        MF0(2,1,2,3); SB();
        MF(2,0,3,2); SB(); ZA(1); SB();
        MF(2,1,3,2); SB(); ZB(1); SB();
        MF(2,0,4,1); SB(); ZC(1); SB();
        MF(2,1,4,1); SB();  SB();
        MF(2,0,5,0); SB(); ZD(1); SB();
        MF(2,1,5,0); SB();
        BARS(0); SB();
        PRIO(2); SB();
        ISSUE1(); SB();
        ER(0); SB();
        ER(1); SB();
        EV(0); SB();
        EV(1); SB();
        PRIO(0); SB();
        MF(2,0,1,4); SB();
        MF(2,1,1,4); SB();
        MF(2,0,0,5); SB();
        MF(2,1,0,5); SB();
        MF0(3,0,2,5); SB();
        MF0(3,1,2,5); SB();
        MF(3,0,3,4); SB(); ZA(2); SB();
        MF(3,1,3,4); SB(); ZB(2); SB();
        MF(3,0,4,3); SB(); ZC(2); SB();
        MF(3,1,4,3); SB();  SB();
        MF(3,0,5,2); SB(); ZD(2); SB();
        MF(3,1,5,2); SB();
        MF(3,0,6,1); SB();
        MF(3,1,6,1); SB();
        MF(3,0,7,0); SB();
        MF(3,1,7,0); SB();
        MF(3,0,1,6); SB();
        MF(3,1,1,6); SB();
        MF(3,0,0,7); SB();
        MF(3,1,0,7); SB();
        BARS(1); SB();
        PRIO(2); SB();
        ER(2); SB();
        EV(2); SB();
        PRIO(0); SB();
        MF0(4,0,2,7); SB();
        MF0(4,1,2,7); SB();
        MF(4,0,3,6); SB(); ZA(3); SB();
        MF(4,1,3,6); SB(); ZB(3); SB();
        MF(4,0,4,5); SB(); ZC(3); SB();
        MF(4,1,4,5); SB();  SB();
        MF(4,0,5,4); SB(); ZD(3); SB();
        MF(4,1,5,4); SB();
        MF(4,0,6,3); SB();
        MF(4,1,6,3); SB();
        MF(4,0,7,2); SB();
        MF(4,1,7,2); SB();
        MF(4,0,8,1); SB();
        MF(4,1,8,1); SB();
        MF(4,0,9,0); SB(); CONSUME1(); SB();
        MF(4,1,9,0); SB();
        MF0(5,0,4,7); SB();
        MF0(5,1,4,7); SB();
        MF(5,0,5,6); SB();
        MF(5,1,5,6); SB();
        MF(5,0,6,5); SB();
        MF(5,1,6,5); SB();
        BARS(2); SB();
        PRIO(2); SB();
        LB(8,0,0); SB();
        LB(8,0,1); SB();
        LB(8,1,0); SB();
        LB(8,1,1); SB();
        LB(9,0,0); SB();
        LB(9,0,1); SB();
        LB(9,1,0); SB();
        LB(9,1,1); SB();
        LB(10,0,0); SB();
        LB(10,0,1); SB();
        LB(10,1,0); SB();
        LB(10,1,1); SB();
        LB(11,0,0); SB();
        LB(11,0,1); SB();
        LB(11,1,0); SB();
        LB(11,1,1); SB();
        ER(3); SB();
        EV(3); SB();
        PRIO(0); SB();
        MF(5,0,7,4); SB();
        MF(5,1,7,4); SB();
        MF(5,0,8,3); SB();
        MF(5,1,8,3); SB();
        MF(5,0,9,2); SB();
        MF(5,1,9,2); SB();
        MF(5,0,10,1); SB();
        MF(5,1,10,1); SB();
        MF(5,0,11,0); SB();
        MF(5,1,11,0); SB();
        MF(4,0,1,8); SB();
        MF(4,1,1,8); SB();
        MF(4,0,0,9); SB();
        MF(4,1,0,9); SB();
        MF(5,0,3,8); SB();
        MF(5,1,3,8); SB();
        MF(5,0,2,9); SB(); ZA(4); SB();
        MF(5,1,2,9); SB(); ZB(4); SB();
        MF(5,0,1,10); SB(); ZC(4); SB();
        MF(5,1,1,10); SB();  SB();
        MF(5,0,0,11); SB(); ZD(4); SB();
        MF(5,1,0,11); SB();
        MF0(6,0,2,11); SB();
        MF0(6,1,2,11); SB();
        MF(6,0,3,10); SB(); ZA(5); SB();
        MF(6,1,3,10); SB(); ZB(5); SB();
        MF(6,0,4,9); SB(); ZC(5); SB();
        MF(6,1,4,9); SB();  SB();
        MF(6,0,5,8); SB(); ZD(5); SB();
        MF(6,1,5,8); SB();
        BARS(3); SB();
        PRIO(2); SB();
        LB(12,0,0); SB();
        LB(12,0,1); SB();
        LB(12,1,0); SB();
        LB(12,1,1); SB();
        LB(13,0,0); SB();
        LB(13,0,1); SB();
        LB(13,1,0); SB();
        LB(13,1,1); SB();
        ER(4); SB();
        ER(5); SB();
        EV(4); SB();
        EV(5); SB();
        PRIO(0); SB();
        MF(6,0,6,7); SB();
        MF(6,1,6,7); SB();
        MF(6,0,7,6); SB();
        MF(6,1,7,6); SB();
        MF(6,0,8,5); SB();
        MF(6,1,8,5); SB();
        MF(6,0,9,4); SB();
        MF(6,1,9,4); SB();
        MF(6,0,10,3); SB();
        MF(6,1,10,3); SB();
        MF(6,0,11,2); SB();
        MF(6,1,11,2); SB();
        MF(6,0,12,1); SB();
        MF(6,1,12,1); SB();
        MF(6,0,13,0); SB();
        MF(6,1,13,0); SB();
        MF(6,0,1,12); SB();
        MF(6,1,1,12); SB();
        MF(6,0,0,13); SB();
        MF(6,1,0,13); SB();
        MF0(7,0,2,13); SB();
        MF0(7,1,2,13); SB();
        MF(7,0,3,12); SB(); ZA(6); SB();
        MF(7,1,3,12); SB(); ZB(6); SB();
        MF(7,0,4,11); SB(); ZC(6); SB();
        MF(7,1,4,11); SB();  SB();
        MF(7,0,5,10); SB(); ZD(6); SB();
        MF(7,1,5,10); SB();
        MF(7,0,6,9); SB();
        MF(7,1,6,9); SB();
        BARS(4); SB();
        PRIO(2); SB();
        LB(14,0,0); SB();
        LB(14,0,1); SB();
        LB(14,1,0); SB();
        LB(14,1,1); SB();
        LB(15,0,0); SB();
        LB(15,0,1); SB();
        LB(15,1,0); SB();
        LB(15,1,1); SB();
        ER(6); SB();
        EV(6); SB();
        PRIO(0); SB();
        MF(7,0,7,8); SB();
        MF(7,1,7,8); SB();
        MF(7,0,8,7); SB();
        MF(7,1,8,7); SB();
        MF(7,0,9,6); SB();
        MF(7,1,9,6); SB();
        MF(7,0,10,5); SB();
        MF(7,1,10,5); SB();
        MF(7,0,11,4); SB();
        MF(7,1,11,4); SB();
        MF(7,0,12,3); SB();
        MF(7,1,12,3); SB();
        MF(7,0,13,2); SB();
        MF(7,1,13,2); SB();
        MF(7,0,14,1); SB();
        MF(7,1,14,1); SB();
        MF(7,0,15,0); SB();
        MF(7,1,15,0); SB();
        MF(7,0,1,14); SB();
        MF(7,1,1,14); SB();
        MF(7,0,0,15); SB();
        MF(7,1,0,15); SB();
        ZA(7); ZB(7); ZC(7);  ZD(7); SB();
        BARS(5); SB();
        ER(7); EV(7);
    }
    if (ISP && blockIdx.x == 0 && tid == 0) __builtin_amdgcn_raw_buffer_store_b32(tag, wsr, WS_E, 0, 16);
    STAMP(10);
}

__global__ void __launch_bounds__(512, 2) fused_kernel(Args a) {
    extern __shared__ __attribute__((aligned(16))) char smem_[];
    const char* pc0 = (const char*)__builtin_amdgcn_s_getpc();
    asm volatile("" :: "s"(a.u), "s"(a.lar), "s"(a.aim), "s"(a.cre), "s"(a.cim), "s"(a.ldt), "s"(a.dskip), "s"(a.kk), "s"(a.vv), "s"(a.shk),
                 "s"(a.ssmk), "s"(a.ssmD), "s"(a.gw), "s"(a.out), "s"(a.ws));
    const int tid = threadIdx.x, wv = __builtin_amdgcn_readfirstlane(tid >> 6);
    if (__builtin_expect(wv < 4, 1)) body<true>(a, (LAS char*)smem_, tid, wv, pc0);
    else body<false>(a, (LAS char*)smem_, tid, wv, pc0);
}

extern "C" void kernel_launch(void* const* d_in, const int* in_sizes, int n_in, void* d_out, int out_size, void* d_ws,
                              size_t ws_size, hipStream_t stream) {
    (void)hipFuncSetAttribute((const void*)fused_kernel, hipFuncAttributeMaxDynamicSharedMemorySize, LDS_BYTES);
    Args a;
    a.u = (const u16*)d_in[0]; a.lar = (const u16*)d_in[1]; a.aim = (const u16*)d_in[2]; a.cre = (const u16*)d_in[3]; a.cim = (const u16*)d_in[4];
    a.ldt = (const u16*)d_in[5]; a.dskip = (const u16*)d_in[6]; a.kk = (const u16*)d_in[8]; a.vv = (const u16*)d_in[9]; a.shk = (const u16*)d_in[10];
    a.ssmk = (const u16*)d_in[11]; a.ssmD = (const u16*)d_in[12]; a.gw = (const u16*)d_in[13]; a.out = (u16*)d_out; a.ws = (char*)d_ws;
    fused_kernel<<<256, 512, LDS_BYTES, stream>>>(a);
}
```

```cpp
#include <hip/hip_runtime.h>
#include <stdint.h>
#include <type_traits>

typedef unsigned short u16;
typedef unsigned v4u __attribute__((ext_vector_type(4)));
typedef unsigned v2u __attribute__((ext_vector_type(2)));
typedef short v4s __attribute__((ext_vector_type(4)));
typedef float v4f __attribute__((ext_vector_type(4)));
typedef float v2f __attribute__((ext_vector_type(2)));
typedef __bf16 v2bf __attribute__((ext_vector_type(2)));
typedef __bf16 v8bf __attribute__((ext_vector_type(8)));
typedef _Float16 v2h __attribute__((ext_vector_type(2)));
typedef _Float16 v8h __attribute__((ext_vector_type(8)));
#define LAS __attribute__((address_space(3)))
typedef __amdgpu_buffer_rsrc_t rsrc_t;

constexpr int XR = 1184;
constexpr int XC = 4 * XR + 8;
constexpr int X_OFF = 0;
constexpr int KS = 4096 + 32;
constexpr int K_OFF = 4 * XC;
constexpr int TR2 = 144, TW = 51 * TR2;
constexpr int S_OFF = K_OFF + 4 * KS;
constexpr int F_OFF = S_OFF + 64;
constexpr int U_OFF = F_OFF + 2048;
constexpr int FG_OFF = U_OFF;
constexpr int FGR = 96;
constexpr int FGS = 64 * FGR + 64;
constexpr int FGC = 8 * FGS;
constexpr int FG_END = FG_OFF + 2 * FGC;
constexpr int YS = 8192 + 256;
constexpr int Y_OFF = U_OFF;
constexpr int LDS_BYTES = FG_END > Y_OFF + 8 * YS ? FG_END : Y_OFF + 8 * YS;
static_assert(LDS_BYTES <= 163840 && U_OFF % 16 == 0 && K_OFF % 16 == 0 && 4 * TW <= S_OFF, "lds map");

constexpr int WS_G = 4 << 20;
constexpr int WS_F = WS_G + 8192;
constexpr int WS_E = WS_F + 32768;
constexpr unsigned PUBT = 140;
constexpr int IDELAY = 7, EDEL = 6;
constexpr unsigned CODE_LINES = 112;

__device__ __forceinline__ float bflo(unsigned x) { return __uint_as_float(x << 16); }
__device__ __forceinline__ float bfhi(unsigned x) { return __uint_as_float(x & 0xffff0000u); }
__device__ __forceinline__ unsigned pkbf(float a, float b) { v2f t = {a, b}; return __builtin_bit_cast(unsigned, __builtin_convertvector(t, v2bf)); }
__device__ __forceinline__ unsigned pkh(v2f t) { return __builtin_bit_cast(unsigned, __builtin_convertvector(t, v2h)); }
__device__ __forceinline__ float rbf(float a) { return bflo(pkbf(a, a)); }
__device__ __forceinline__ float dot2(unsigned a, unsigned b, float c) {
    return __builtin_amdgcn_fdot2_f32_bf16(__builtin_bit_cast(v2bf, a), __builtin_bit_cast(v2bf, b), c, false);
}
__device__ __forceinline__ v2u trread(LAS char* p) { return __builtin_bit_cast(v2u, __builtin_amdgcn_ds_read_tr16_b64_v4i16((LAS v4s*)p)); }
template <int CTRL, int RM> __device__ __forceinline__ float dppf(float x) {
    return __int_as_float(__builtin_amdgcn_update_dpp(0, __float_as_int(x), CTRL, RM, 0xf, false));
}
template <int CTRL, int RM> __device__ __forceinline__ unsigned dppu(unsigned x) {
    return (unsigned)__builtin_amdgcn_update_dpp(0, (int)x, CTRL, RM, 0xf, false);
}
__device__ __forceinline__ float exp_acc(float x) {
    const float t = x * 1.44269502162933349609375f;
    const float lo = __builtin_fmaf(x, 1.92596299112661746e-8f, __builtin_fmaf(x, 1.44269502162933349609375f, -t));
    const float e = __builtin_amdgcn_exp2f(t);
    return __builtin_fmaf(e, lo * 0.693147182464599609375f, e);
}
struct cst { v2f b, p; };
__device__ __forceinline__ v2f cmul(v2f a, cst c) { return a.xx * c.b + a.yy * c.p; }
__device__ __forceinline__ cst cexp_p(float zr, float zi, float p) {
    const float e = __builtin_amdgcn_exp2f(zr * p), t = __builtin_amdgcn_fractf(zi * p);
    const float c = e * __builtin_amdgcn_cosf(t), s = e * __builtin_amdgcn_sinf(t);
    cst r; r.b = (v2f){c, s}; r.p = (v2f){-s, c};
    return r;
}
#define SB() __builtin_amdgcn_sched_barrier(0)
#define BAR() do { asm volatile("s_waitcnt lgkmcnt(0)\n\ts_barrier" ::: "memory"); } while (0)
#define PIN(x) asm volatile("" : "+v"(x))
#define VMWAIT(n) do { SB(); asm volatile("s_waitcnt vmcnt(" #n ")" ::: "memory"); SB(); } while (0)


struct Buf { void* p; int n; int f; };
struct __attribute__((aligned(16))) Args {
    Buf ub, kk, vb, ws, out;
    const u16 *lar, *aim, *cre, *cim, *ldt, *dskip, *shk, *ssmk, *ssmD, *gw;
};
typedef unsigned v16u __attribute__((ext_vector_type(16)));
typedef unsigned v8u __attribute__((ext_vector_type(8)));
#define GAS __attribute__((address_space(1)))
__device__ __forceinline__ unsigned long long mk64(unsigned lo, unsigned hi) { return (unsigned long long)lo | ((unsigned long long)hi << 32); }
__global__ void __launch_bounds__(512, 2) fused_kernel(Args args_) {
    extern __shared__ __attribute__((aligned(16))) char smem_[];
    LAS char* const sm = (LAS char*)smem_;
    unsigned long long tstart;
    v16u kA, kB; v8u kC;
    asm volatile("s_load_dwordx16 %0, %3, 0x0\n\ts_load_dwordx16 %1, %3, 0x40\n\ts_load_dwordx8 %2, %3, 0x80"
                 : "=&s"(kA), "=&s"(kB), "=&s"(kC) : "s"(__builtin_amdgcn_kernarg_segment_ptr()) : "memory");
    const int tid0 = threadIdx.x ^ 256, wv = __builtin_amdgcn_readfirstlane(tid0 >> 6);
    const int gam = blockIdx.x & 7, m = blockIdx.x >> 3;
    const int d0 = 64 * gam + 2 * m;
    const int Jp = wv & 1, chalf = (wv >> 1) & 1;
    const int pc = d0 + chalf;
    unsigned p_ldt = 0, p_lar = 0, p_aim = 0, p_cre = 0, p_cim = 0, m_re, m_im, cv;
    v4u t_u[2], t_k[2], t_v[2], t_h;
    v2u shv[2];
    unsigned tag0;
    {
        {
            const unsigned long long pcb = __builtin_amdgcn_s_getpc();
            const unsigned co = 128u * min(64u * (m & 1) + (unsigned)(tid0 >> 3), CODE_LINES - 1u);
            asm volatile("global_load_dword %0, %1, %2" : "=&v"(cv) : "v"(co), "s"(pcb) : "memory");
            if (wv >= 4) __builtin_amdgcn_s_setprio(3);
        }
        *(LAS unsigned*)(sm + S_OFF + 32 + 4 * wv) = 0u;
        SB();
        const unsigned toff = ((64 * m + 16 * wv) * 1024 + 128 * gam) + (((tid0 & 56) << 7) | ((tid0 & 7) << 4));
        const unsigned hoff = (tid0 & 63) < 24 ? toff - 3u * 1024u : 0xfffffff0u;
        const unsigned po = pc * 256 + 2 * (tid0 & 127), lo = 2 * pc, mo = pc * 256 + 4 * (tid0 & 63);
        const unsigned sho = 512 * gam + (((tid0 & 32) << 2) | ((tid0 & 15) << 3)), eo = WS_E;
        asm volatile("" :: "v"(sho), "v"(eo), "v"(toff), "v"(hoff), "v"(po), "v"(lo), "v"(mo));
        asm volatile("" : "+v"(p_ldt), "+v"(p_lar), "+v"(p_aim), "+v"(p_cre), "+v"(p_cim));
        SB();
        asm volatile("s_waitcnt lgkmcnt(0)" : "+s"(kA), "+s"(kB), "+s"(kC) :: "memory");
        tstart = __builtin_amdgcn_s_memrealtime();
        SB();
        asm volatile(
            "s_cmp_lt_u32 %[wv], 4\n\ts_cbranch_scc1 2f\n\t"
            "global_load_short_d16_hi %[ldt], %[lo], %[pldt]\n\t"
            "global_load_short_d16_hi %[lar], %[po], %[plar]\n\t"
            "global_load_short_d16_hi %[aim], %[po], %[paim]\n\t"
            "global_load_short_d16_hi %[cre], %[po], %[pcre]\n\t"
            "global_load_short_d16_hi %[cim], %[po], %[pcim]\n\t"
            "global_load_dword %[mre], %[mo], %[pcre]\n\t"
            "global_load_dword %[mim], %[mo], %[pcim]\n"
            "2:"
            : [mre] "=&v"(m_re), [mim] "=&v"(m_im), [ldt] "+v"(p_ldt), [lar] "+v"(p_lar), [aim] "+v"(p_aim), [cre] "+v"(p_cre), [cim] "+v"(p_cim)
            : [wv] "s"(wv), [po] "v"(po), [mo] "v"(mo), [lo] "v"(lo),
              [pldt] "s"(kB.scd), [plar] "s"(kB.s45), [paim] "s"(kB.s67), [pcre] "s"(kB.s89), [pcim] "s"(kB.sab)
            : "memory", "scc");
        asm volatile(
            "s_cmp_lt_u32 %[wv], 4\n\ts_cbranch_scc0 1f\n\t"
            "global_load_dwordx2 %[s0], %[sho], %[pshk]\n\t"
            "global_load_dwordx2 %[s1], %[sho], %[pshk] offset:256\n\t"
            "global_load_dword %[tag], %[eo], %[pws] sc1\n\t"
            "buffer_load_dwordx4 %[k0], %[toff], %[rk], 0 offen\n\t"
            "buffer_load_dwordx4 %[k1], %[toff], %[rk], %[c8k] offen\n\t"
            "buffer_load_dwordx4 %[h], %[hoff], %[rk], 0 offen\n\t"
            "buffer_load_dwordx4 %[v0], %[toff], %[rv], 0 offen\n\t"
            "buffer_load_dwordx4 %[v1], %[toff], %[rv], %[c8k] offen\n\t"
            "buffer_load_dwordx4 %[u0], %[toff], %[ru], 0 offen\n\t"
            "buffer_load_dwordx4 %[u1], %[toff], %[ru], %[c8k] offen\n"
            "1:"
            : [u0] "=&v"(t_u[0]), [u1] "=&v"(t_u[1]), [k0] "=&v"(t_k[0]), [k1] "=&v"(t_k[1]), [v0] "=&v"(t_v[0]), [v1] "=&v"(t_v[1]), [h] "=&v"(t_h),
              [s0] "=&v"(shv[0]), [s1] "=&v"(shv[1]), [tag] "=&v"(tag0)
            : [wv] "s"(wv), [toff] "v"(toff), [hoff] "v"(hoff), [ru] "s"(kA.s0123), [rk] "s"(kA.s4567), [rv] "s"(kA.s89ab), [c8k] "s"(8192),
              [sho] "v"(sho), [eo] "v"(eo), [pshk] "s"(kC.s01), [pws] "s"(kA.scd)
            : "memory", "scc");
    }
    const rsrc_t wsr = __builtin_amdgcn_make_buffer_rsrc((void*)mk64(kA.sc, kA.sd), 0, (int)kA.se, (int)kA.sf);
    const GAS char* const p_u = (const GAS char*)mk64(kA.s0, kA.s1);
    const GAS char* const p_dskip = (const GAS char*)mk64(kB.se, kB.sf);
    const GAS char* const p_ssmk = (const GAS char*)mk64(kC.s2, kC.s3);
    const GAS char* const p_ssmD = (const GAS char*)mk64(kC.s4, kC.s5);
    const GAS char* const p_gw = (const GAS char*)mk64(kC.s6, kC.s7);
    SB();
    auto body = [&](auto role) __attribute__((always_inline)) {
    constexpr bool isP = decltype(role)::value;
    int tid = tid0;
    if (isP) asm volatile("; P" : "+v"(tid)); else asm volatile("; K" : "+v"(tid));
    const int lane = tid & 63, t8 = tid & 255;
    const int q = (lane >> 2) & 3, pp = lane & 3, i16 = lane & 15, rp = (lane >> 4) & 1, hi = lane >> 5, kb = lane >> 4;
    unsigned tag, fl;
    v4u x0, x1;
    int ib, fo;
    LAS char *xp, *fw;
    auto addr_inbox = [&]() {
        ib = (gam * 32 + m) * 8192 + (isP ? 0 : 2 << 20) + 16 * t8;
        fo = WS_F + (32 * gam + (isP ? 0 : 16) + 2 * (wv & 3) + (lane & 1) + 4 * (lane & 2)) * 128;
        xp = sm + X_OFF + (2 * (tid & 1) + ((tid >> 2) & 1)) * XC + (tid & 2) * XR + 128 + (isP ? 0 : 512) + 8 * (t8 >> 3);
        fw = sm + F_OFF + (isP ? 0 : 1024) + 16 * lane + 4 * (wv & 3);
        PIN(ib); PIN(fo); PIN(xp); PIN(fw);
    };
#define UNTIL(T) do { SB(); _Pragma("nounroll") for (int z_ = 0; z_ < 64 && (unsigned)(__builtin_amdgcn_s_memrealtime() - tstart) < (T); ++z_) __builtin_amdgcn_s_sleep(1); SB(); } while (0)
#define POLL() fl = __builtin_amdgcn_raw_buffer_load_b32(wsr, fo, 0, 16)
#define READY() do { for (unsigned spins = 0; !__all(fl == tag) && spins < (1u << 16); ++spins) { \
            __builtin_amdgcn_s_sleep(4); asm volatile("" ::: "memory"); POLL(); } \
        asm volatile("" ::: "memory"); } while (0)
#define INBOX() do { x0 = __builtin_amdgcn_raw_buffer_load_b128(wsr, ib, 0, 16); x1 = __builtin_amdgcn_raw_buffer_load_b128(wsr, ib, 4096, 16); } while (0)
#define TAKE() do { *(LAS v2u*)(xp) = x0.xy; *(LAS v2u*)(xp + XR) = x0.zw; *(LAS v2u*)(xp + 256) = x1.xy; *(LAS v2u*)(xp + 256 + XR) = x1.zw; } while (0)
#define WAITF(off, lim) for (;;) { const v4u f = *(volatile LAS v4u*)(sm + F_OFF + (off)); \
            if (__builtin_amdgcn_readfirstlane(min(min(f.x, f.y), min(f.z, f.w))) > (unsigned)(lim)) break; \
            __builtin_amdgcn_s_sleep(1); }
    if (isP) {
        const int J = 4 * m + wv;
        LAS char* const tw = sm + wv * TW;
        LAS char* tp = tw + (lane >> 3) * TR2 + 16 * (lane & 7);
        LAS char* tb = tw + (2 * rp + 4 * q) * TR2 + 32 * hi + 8 * pp;
        LAS char* tk = tb - 3 * q * TR2;
        int so[4];
        so[0] = ((J >> 6) << 21) + ((gam * 32 + (i16 >> 1)) * 64 + (J & 63)) * 128 + 32 * kb + 16 * (i16 & 1);
#pragma unroll
        for (int z = 1; z < 4; ++z) so[z] = so[0] + z * 8 * 8192;
        PIN(tp); PIN(tb); PIN(tk); PIN(so[0]); PIN(so[1]); PIN(so[2]); PIN(so[3]);
        addr_inbox();
        LAS char* ka = sm + K_OFF + wv * KS + 32 + 32 * kb + 8 * (pp - q);
        LAS char* xa = sm + X_OFF + wv * XC + 128 - 96 + 8 * (i16 - kb);
        const int Q = 4 * i16 + kb;
        LAS char* yw = sm + Y_OFF + wv * 2 * YS + 64 + 16 * (Q ^ ((Q >> 3) & 3));
        LAS char* z0 = sm + X_OFF + wv * XC + kb * XR + 8 * i16;
        LAS char* z1 = sm + K_OFF + wv * KS + 8 * pp;
        PIN(ka); PIN(xa); PIN(yw); PIN(z0); PIN(z1);
        __builtin_amdgcn_s_setprio(1);
        SB();
        asm volatile("s_barrier" ::: "memory");
        VMWAIT(4);
        asm volatile("" : "+v"(shv[0]), "+v"(shv[1]), "+v"(tag0) : "v"(cv));
        tag = tag0 * 0x9E3779B1u + 0x7F4A7C15u;
        PIN(tag);
        asm volatile("" : "+v"(t_k[0]), "+v"(t_k[1]), "+v"(t_h));
#pragma unroll
        for (int z = 0; z < 2; ++z) *(LAS v4u*)(tp + (35 + 8 * z) * TR2) = t_k[z];
        if (lane < 24) *(LAS v4u*)(tp + 32 * TR2) = t_h;
        asm volatile("" ::: "memory");
        v2u uc[2][2], vc[2][2], kq[2][2][4];
        float o[2][2][4];
#pragma unroll
        for (int c = 0; c < 2; ++c) {
#pragma unroll
            for (int e = 0; e < 4; ++e) { kq[c][0][e] = trread(tk + (32 + 4 * e) * TR2 + 64 * c); kq[c][1][e] = trread(tk + (33 + 4 * e) * TR2 + 64 * c); }
        }
        SB();
#pragma unroll
        for (int c = 0; c < 2; ++c) {
            unsigned s32 = __builtin_amdgcn_alignbit(shv[c].y, shv[c].y, 16), s10 = __builtin_amdgcn_alignbit(shv[c].x, shv[c].x, 16);
            PIN(s32); PIN(s10);
#pragma unroll
            for (int z = 0; z < 2; ++z)
                asm volatile("v_dot2_f32_bf16 %0, %4, %12, 0\n\tv_dot2_f32_bf16 %1, %6, %12, 0\n\tv_dot2_f32_bf16 %2, %8, %12, 0\n\tv_dot2_f32_bf16 %3, %10, %12, 0\n\t"
                             "v_dot2_f32_bf16 %0, %5, %13, %0\n\tv_dot2_f32_bf16 %1, %7, %13, %1\n\tv_dot2_f32_bf16 %2, %9, %13, %2\n\tv_dot2_f32_bf16 %3, %11, %13, %3\n\ts_nop 2"
                             : "=&v"(o[c][z][0]), "=&v"(o[c][z][1]), "=&v"(o[c][z][2]), "=&v"(o[c][z][3])
                             : "v"(kq[c][z][0].x), "v"(kq[c][z][0].y), "v"(kq[c][z][1].x), "v"(kq[c][z][1].y), "v"(kq[c][z][2].x), "v"(kq[c][z][2].y),
                               "v"(kq[c][z][3].x), "v"(kq[c][z][3].y), "v"(s32), "v"(s10));
        }
        VMWAIT(2);
        asm volatile("" : "+v"(t_v[0]), "+v"(t_v[1]));
#pragma unroll
        for (int z = 0; z < 2; ++z) *(LAS v4u*)(tp + (16 + 8 * z) * TR2) = t_v[z];
        asm volatile("" ::: "memory");
#pragma unroll
        for (int c = 0; c < 2; ++c) { vc[c][0] = trread(tb + 16 * TR2 + 64 * c); vc[c][1] = trread(tb + 17 * TR2 + 64 * c); }
        SB();
        unsigned kvp[2][4];
#pragma unroll
        for (int c = 0; c < 2; ++c) {
            kvp[c][0] = pkbf(o[c][0][0] * bflo(vc[c][0].x), o[c][0][1] * bfhi(vc[c][0].x));
            kvp[c][1] = pkbf(o[c][0][2] * bflo(vc[c][0].y), o[c][0][3] * bfhi(vc[c][0].y));
            kvp[c][2] = pkbf(o[c][1][0] * bflo(vc[c][1].x), o[c][1][1] * bfhi(vc[c][1].x));
            kvp[c][3] = pkbf(o[c][1][2] * bflo(vc[c][1].y), o[c][1][3] * bfhi(vc[c][1].y));
            PIN(kvp[c][0]); PIN(kvp[c][1]); PIN(kvp[c][2]); PIN(kvp[c][3]);
        }
        VMWAIT(0);
        asm volatile("" : "+v"(t_u[0]), "+v"(t_u[1]));
#pragma unroll
        for (int z = 0; z < 2; ++z) *(LAS v4u*)(tp + 8 * z * TR2) = t_u[z];
        asm volatile("" ::: "memory");
#pragma unroll
        for (int c = 0; c < 2; ++c) { uc[c][0] = trread(tb + 64 * c); uc[c][1] = trread(tb + TR2 + 64 * c); }
        SB();
        v4u pay[4];
#pragma unroll
        for (int c = 0; c < 2; ++c) {
            const auto w0 = __builtin_amdgcn_permlane32_swap(uc[c][0].x, kvp[c][0], false, false);
            const auto w1 = __builtin_amdgcn_permlane32_swap(uc[c][0].y, kvp[c][1], false, false);
            const auto w2 = __builtin_amdgcn_permlane32_swap(uc[c][1].x, kvp[c][2], false, false);
            const auto w3 = __builtin_amdgcn_permlane32_swap(uc[c][1].y, kvp[c][3], false, false);
            pay[2 * c] = (v4u){w0[0], w1[0], w2[0], w3[0]}; pay[2 * c + 1] = (v4u){w0[1], w1[1], w2[1], w3[1]};
            PIN(pay[2 * c]); PIN(pay[2 * c + 1]);
        }
        UNTIL(PUBT);
#pragma unroll
        for (int z = 0; z < 4; ++z) __builtin_amdgcn_raw_buffer_store_b128(pay[z], wsr, so[z], 0, 16);
        SB();
        VMWAIT(0);
        *(volatile LAS unsigned*)(sm + S_OFF + 32 + 4 * wv) = 1u;
        {
            const v4u ar = *(volatile LAS v4u*)(sm + S_OFF + 32);
            __builtin_amdgcn_raw_buffer_store_b32(tag, wsr, (lane == 0 && (ar.x & ar.y & ar.z & ar.w)) ? WS_F + (32 * gam + m) * 128 : -16, 0, 16);
        }
        SB();
        BAR();
        *(LAS v2u*)z0 = (v2u){0u, 0u}; *(LAS v2u*)z1 = (v2u){0u, 0u};
        for (int z = 0; z < IDELAY; ++z) __builtin_amdgcn_s_sleep(2);
        POLL();
        READY();
        INBOX();
        SB();
        __builtin_amdgcn_s_setprio(2);
        WAITF(1024, 0);
        asm volatile("" ::: "memory");
        v2u A[16][2];
#define LA(s, hf) A[s][hf] = trread(ka + 256 * (s) + 128 * (hf))
#pragma unroll
        for (int s = 0; s < 8; ++s) { LA(s, 0); LA(s, 1); }
        if (lane < 4) *(LAS v4u*)(fw + (Y_OFF - F_OFF + YS) + wv * (2 * YS - 4)) = (v4u){0u, 0u, 0u, 0u};
        SB();
        TAKE();
        asm volatile("" ::: "memory");
        *(volatile LAS unsigned*)fw = 1u;
        for (;;) { const v4u f = *(volatile LAS v4u*)(sm + F_OFF); if (__builtin_amdgcn_readfirstlane(min(min(f.x, f.y), min(f.z, f.w))) != 0u) break; }
        asm volatile("" ::: "memory");
        v4f acc[8][4], zl[8], zw[8];
        v2u Br[8][4][2], Bs[72][4][2];
        v4u s1f;
#define LB(V, idx, r, hf) V[r][hf] = *(volatile LAS v2u*)(xa + (r) * XR + 64 * (idx) + 32 - 32 * (hf))
#define FRAG(F_) __builtin_bit_cast(v8bf, (v4u){F_[0].x, F_[0].y, F_[1].x, F_[1].y})
#define MF(t, r, s, V) acc[t][r] = __builtin_amdgcn_mfma_f32_16x16x32_bf16(FRAG(A[s]), FRAG(V[r]), acc[t][r], 0, 0, 0)
#define MF0(t, r, s, V) acc[t][r] = __builtin_amdgcn_mfma_f32_16x16x32_bf16(FRAG(A[s]), FRAG(V[r]), (v4f){0.f, 0.f, 0.f, 0.f}, 0, 0, 0)
#define ADDP(d, x, y) do { float z_ = (x) + (y); PIN(z_); d = z_; } while (0)
#define Z0(t) do { zl[t] = acc[t][0]; zw[t] = acc[t][3]; ADDP(zl[t][1], zl[t][1], acc[t][1][0]); } while (0)
#define Z1(t) ADDP(zl[t][2], zl[t][2], acc[t][1][1])
#define Z2(t) ADDP(zl[t][3], zl[t][3], acc[t][1][2])
#define Z3(t) ADDP(zl[t][2], zl[t][2], acc[t][2][0])
#define Z4(t) ADDP(zl[t][3], zl[t][3], acc[t][2][1])
#define Z5(t) ADDP(zw[t][1], zw[t][1], acc[t][1][3])
#define Z6(t) ADDP(zw[t][2], zw[t][2], acc[t][2][3])
#define Z7(t) ADDP(zw[t][1], zw[t][1], acc[t][2][2])
#define ZL(t) *(volatile LAS v4f*)(yw + 1024 * (t)) = zl[t]
#define ZW(t) *(volatile LAS v4f*)(yw + 1024 * (t) + YS) = zw[t]
#define ZF(t) do { *(volatile LAS unsigned*)fw = (t) + 2; } while (0)
#define ZK(t) asm volatile("" :: "v"(zl[t]), "v"(zw[t]))
#define S1READ() s1f = *(volatile LAS v4u*)(sm + F_OFF + 1024)
#define S1CHECK() do { unsigned t0_ = s1f.x & s1f.y & s1f.z & s1f.w, t1_, t2_, t3_, sc_; unsigned fa_ = (unsigned)(size_t)(sm + F_OFF + 1024); \
        asm volatile("s_nop 0\n1:\n\tv_readfirstlane_b32 %4, %0\n\ts_bitcmp1_b32 %4, 1\n\ts_cbranch_scc1 2f\n\t" \
                     "ds_read_b32 %0, %5\n\tds_read_b32 %1, %5 offset:4\n\tds_read_b32 %2, %5 offset:8\n\tds_read_b32 %3, %5 offset:12\n\ts_waitcnt lgkmcnt(0)\n\t" \
                     "v_and_b32 %0, %0, %1\n\tv_and_b32 %2, %2, %3\n\tv_and_b32 %0, %0, %2\n\ts_nop 0\n\ts_branch 1b\n2:" \
                     : "+v"(t0_), "=&v"(t1_), "=&v"(t2_), "=&v"(t3_), "=&s"(sc_) : "v"(fa_) : "memory", "scc"); } while (0)
        LB(Br[0],0,0,0); LB(Br[0],0,0,1); LB(Br[0],0,1,0); LB(Br[0],0,1,1); LB(Br[0],0,2,0); LB(Br[0],0,2,1); LB(Br[0],0,3,0); LB(Br[0],0,3,1); LB(Br[1],1,3,0); LB(Br[1],1,3,1); LB(Br[1],1,2,0); LB(Br[1],1,2,1); LB(Br[1],1,1,0); LB(Br[1],1,1,1); LB(Br[1],1,0,0); LB(Br[1],1,0,1); LB(Br[2],2,3,0); LB(Br[2],2,3,1); SB();
        MF0(0,0,1,Br[0]); SB(); LB(Br[2],2,2,0); SB();
        MF0(0,1,1,Br[0]); SB(); LB(Br[2],2,2,1); SB();
        MF0(0,2,1,Br[0]); SB(); LB(Br[2],2,1,0); SB();
        MF0(0,3,1,Br[0]); SB(); LB(Br[2],2,1,1); SB();
        MF(0,0,0,Br[1]); SB(); LB(Br[2],2,0,0); SB();
        MF(0,1,0,Br[1]); SB(); LB(Br[2],2,0,1); SB();
        MF(0,2,0,Br[1]); SB(); LB(Br[3],3,3,0); SB();
        MF(0,3,0,Br[1]); SB(); LB(Br[3],3,3,1); SB();
        MF0(1,0,3,Br[0]); SB(); LB(Br[3],3,2,0); SB();
        MF0(1,1,3,Br[0]); SB(); LB(Br[3],3,2,1); SB();
        MF0(1,2,3,Br[0]); SB(); LB(Br[3],3,1,0); Z0(0); SB();
        MF0(1,3,3,Br[0]); SB(); LB(Br[3],3,1,1); Z1(0); SB();
        MF(1,0,2,Br[1]); SB(); LB(Br[3],3,0,0); Z2(0); SB();
        MF(1,1,2,Br[1]); SB(); LB(Br[3],3,0,1); Z3(0); SB();
        MF(1,2,2,Br[1]); SB(); LB(Br[4],4,3,0); Z4(0); SB();
        MF(1,3,2,Br[1]); SB(); Z5(0); ZL(0); SB();
        MF(1,0,1,Br[2]); SB(); Z6(0); SB();
        MF(1,1,1,Br[2]); SB(); LB(Br[4],4,3,1); Z7(0); SB();
        MF(1,2,1,Br[2]); SB(); ZW(0); SB();
        MF(1,3,1,Br[2]); SB();
        MF(1,0,0,Br[3]); SB(); ZF(0); SB();
        MF(1,1,0,Br[3]); SB(); LB(Br[4],4,2,0); SB();
        MF(1,2,0,Br[3]); SB(); LB(Br[4],4,2,1); ZK(0); SB();
        MF(1,3,0,Br[3]); SB(); LB(Br[4],4,1,0); SB();
        MF0(2,0,5,Br[0]); SB(); LB(Br[4],4,1,1); SB();
        MF0(2,1,5,Br[0]); SB(); LB(Br[4],4,0,0); SB();
        MF0(2,2,5,Br[0]); SB(); LB(Br[4],4,0,1); Z0(1); SB();
        MF0(2,3,5,Br[0]); SB(); LB(Br[5],5,3,0); Z1(1); SB();
        MF(2,0,4,Br[1]); SB(); LB(Br[5],5,3,1); Z2(1); SB();
        MF(2,1,4,Br[1]); SB(); LB(Br[5],5,2,0); Z3(1); SB();
        MF(2,2,4,Br[1]); SB(); LB(Br[5],5,2,1); Z4(1); SB();
        MF(2,3,4,Br[1]); SB(); Z5(1); ZL(1); SB();
        MF(2,0,3,Br[2]); SB(); Z6(1); SB();
        MF(2,1,3,Br[2]); SB(); LB(Br[5],5,1,0); Z7(1); SB();
        MF(2,2,3,Br[2]); SB(); ZW(1); SB();
        MF(2,3,3,Br[2]); SB();
        MF(2,0,2,Br[3]); SB(); ZF(1); SB();
        MF(2,1,2,Br[3]); SB(); LB(Br[5],5,1,1); SB();
        MF(2,2,2,Br[3]); SB(); LB(Br[5],5,0,0); ZK(1); SB();
        MF(2,3,2,Br[3]); SB(); LB(Br[5],5,0,1); SB();
        MF(2,0,1,Br[4]); SB();
        MF(2,1,1,Br[4]); SB();
        MF(2,2,1,Br[4]); SB();
        MF(2,3,1,Br[4]); SB(); LB(Bs[14],6,3,0); SB();
        MF(2,0,0,Br[5]); SB(); LB(Bs[14],6,3,1); SB();
        MF(2,1,0,Br[5]); SB(); LB(Bs[14],6,2,0); SB();
        MF(2,2,0,Br[5]); SB(); LB(Bs[14],6,2,1); SB();
        MF(2,3,0,Br[5]); SB(); LB(Bs[14],6,1,0); SB();
        MF0(3,0,7,Br[0]); SB(); LB(Bs[14],6,1,1); SB();
        MF0(3,1,7,Br[0]); SB(); LB(Bs[14],6,0,0); SB();
        MF0(3,2,7,Br[0]); SB(); LB(Bs[14],6,0,1); Z0(2); SB();
        MF0(3,3,7,Br[0]); SB(); LB(Bs[17],7,3,0); Z1(2); SB();
        MF(3,0,6,Br[1]); SB(); LB(Bs[17],7,3,1); Z2(2); SB();
        MF(3,1,6,Br[1]); SB(); LB(Bs[17],7,2,0); Z3(2); SB();
        MF(3,2,6,Br[1]); SB(); LB(Bs[17],7,2,1); Z4(2); SB();
        MF(3,3,6,Br[1]); SB(); Z5(2); ZL(2); SB();
        MF(3,0,1,Bs[14]); SB(); Z6(2); SB();
        MF(3,1,1,Bs[14]); SB(); LB(Bs[17],7,1,0); Z7(2); SB();
        MF(3,2,1,Bs[14]); SB(); ZW(2); SB();
        MF(3,3,1,Bs[14]); SB();
        MF(3,0,5,Br[2]); SB(); ZF(2); SB();
        MF(3,1,5,Br[2]); SB(); LB(Bs[17],7,1,1); SB();
        MF(3,2,5,Br[2]); SB(); LB(Bs[17],7,0,0); ZK(2); SB();
        MF(3,3,5,Br[2]); SB(); LB(Bs[17],7,0,1); SB();
        MF(3,0,4,Br[3]); SB();
        MF(3,1,4,Br[3]); SB();
        MF(3,2,4,Br[3]); SB();
        MF(3,3,4,Br[3]); SB();
        MF(3,0,0,Bs[17]); SB();
        MF(3,1,0,Bs[17]); SB();
        MF(3,2,0,Bs[17]); SB();
        MF(3,3,0,Bs[17]); SB();
        MF(3,0,3,Br[4]); SB();
        MF(3,1,3,Br[4]); SB();
        MF(3,2,3,Br[4]); SB(); LA(9,0); SB();
        MF(3,3,3,Br[4]); SB(); LA(9,1); SB();
        MF(3,0,2,Br[5]); SB();
        MF(3,1,2,Br[5]); SB();
        MF(3,2,2,Br[5]); SB(); LA(8,0); SB();
        MF(3,3,2,Br[5]); SB(); LA(8,1); SB();
        MF0(4,0,9,Br[0]); SB(); LB(Bs[24],6,3,0); SB();
        MF0(4,1,9,Br[0]); SB(); LB(Bs[24],6,3,1); SB();
        MF0(4,2,9,Br[0]); SB(); LB(Bs[24],6,2,0); Z0(3); SB();
        MF0(4,3,9,Br[0]); SB(); LB(Bs[24],6,2,1); Z1(3); SB();
        MF(4,0,8,Br[1]); SB(); LB(Bs[24],6,1,0); Z2(3); SB();
        MF(4,1,8,Br[1]); SB(); LB(Bs[24],6,1,1); Z3(3); SB();
        MF(4,2,8,Br[1]); SB(); LB(Bs[24],6,0,0); Z4(3); SB();
        MF(4,3,8,Br[1]); SB(); Z5(3); ZL(3); SB();
        MF(4,0,7,Br[2]); SB(); Z6(3); SB();
        MF(4,1,7,Br[2]); SB(); LB(Bs[24],6,0,1); Z7(3); SB();
        MF(4,2,7,Br[2]); SB(); ZW(3); SB();
        MF(4,3,7,Br[2]); SB();
        MF(4,0,6,Br[3]); SB(); ZF(3); SB();
        MF(4,1,6,Br[3]); SB();
        MF(4,2,6,Br[3]); SB(); ZK(3); SB();
        MF(4,3,6,Br[3]); SB();
        MF(4,0,3,Bs[24]); SB(); LB(Bs[26],7,3,0); SB();
        MF(4,1,3,Bs[24]); SB(); LB(Bs[26],7,3,1); SB();
        MF(4,2,3,Bs[24]); SB(); LB(Bs[26],7,2,0); SB();
        MF(4,3,3,Bs[24]); SB(); LB(Bs[26],7,2,1); SB();
        MF0(5,0,5,Bs[24]); SB(); LB(Bs[26],7,1,0); SB();
        MF0(5,1,5,Bs[24]); SB(); LB(Bs[26],7,1,1); SB();
        MF0(5,2,5,Bs[24]); SB(); LB(Bs[26],7,0,0); SB();
        MF0(5,3,5,Bs[24]); SB(); LB(Bs[26],7,0,1); SB();
        MF(4,0,5,Br[4]); SB(); S1READ(); SB();
        MF(4,1,5,Br[4]); SB();
        MF(4,2,5,Br[4]); SB();
        MF(4,3,5,Br[4]); SB(); S1CHECK(); SB();
        MF(4,0,2,Bs[26]); SB(); LB(Bs[28],8,3,0); SB();
        MF(4,1,2,Bs[26]); SB(); LB(Bs[28],8,3,1); SB();
        MF(4,2,2,Bs[26]); SB(); LB(Bs[28],8,2,0); SB();
        MF(4,3,2,Bs[26]); SB(); LB(Bs[28],8,2,1); SB();
        MF(5,0,4,Bs[26]); SB(); LB(Bs[28],8,1,0); SB();
        MF(5,1,4,Bs[26]); SB(); LB(Bs[28],8,1,1); SB();
        MF(5,2,4,Bs[26]); SB(); LB(Bs[28],8,0,0); SB();
        MF(5,3,4,Bs[26]); SB(); LB(Bs[28],8,0,1); SB();
        MF(4,0,4,Br[5]); SB(); LB(Bs[29],9,3,0); SB();
        MF(4,1,4,Br[5]); SB(); LB(Bs[29],9,3,1); SB();
        MF(4,2,4,Br[5]); SB(); LB(Bs[29],9,2,0); SB();
        MF(4,3,4,Br[5]); SB(); LB(Bs[29],9,2,1); SB();
        MF(4,0,1,Bs[28]); SB(); LB(Bs[29],9,1,0); SB();
        MF(4,1,1,Bs[28]); SB(); LB(Bs[29],9,1,1); SB();
        MF(4,2,1,Bs[28]); SB(); LB(Bs[29],9,0,0); SB();
        MF(4,3,1,Bs[28]); SB(); LB(Bs[29],9,0,1); SB();
        MF(5,0,3,Bs[28]); SB();
        MF(5,1,3,Bs[28]); SB();
        MF(5,2,3,Bs[28]); SB(); LA(11,0); SB();
        MF(5,3,3,Bs[28]); SB(); LA(11,1); SB();
        MF(4,0,0,Bs[29]); SB(); LB(Bs[31],10,3,0); SB();
        MF(4,1,0,Bs[29]); SB(); LB(Bs[31],10,3,1); SB();
        MF(4,2,0,Bs[29]); SB(); LB(Bs[31],10,2,0); SB();
        MF(4,3,0,Bs[29]); SB(); LB(Bs[31],10,2,1); SB();
        MF(5,0,2,Bs[29]); SB(); LB(Bs[31],10,1,0); SB();
        MF(5,1,2,Bs[29]); SB(); LB(Bs[31],10,1,1); SB();
        MF(5,2,2,Bs[29]); SB(); LB(Bs[31],10,0,0); Z0(4); SB();
        MF(5,3,2,Bs[29]); SB(); LB(Bs[31],10,0,1); Z1(4); SB();
        MF(5,0,11,Br[0]); SB(); Z2(4); Z3(4); SB();
        MF(5,1,11,Br[0]); SB(); LA(10,0); Z4(4); SB();
        MF(5,2,11,Br[0]); SB(); LA(10,1); Z5(4); SB();
        MF(5,3,11,Br[0]); SB(); Z6(4); ZL(4); SB();
        MF(5,0,1,Bs[31]); SB(); Z7(4); SB();
        MF(5,1,1,Bs[31]); SB(); LB(Bs[35],11,3,0); SB();
        MF(5,2,1,Bs[31]); SB(); ZW(4); SB();
        MF(5,3,1,Bs[31]); SB();
        MF(5,0,10,Br[1]); SB(); ZF(4); SB();
        MF(5,1,10,Br[1]); SB(); LB(Bs[35],11,3,1); SB();
        MF(5,2,10,Br[1]); SB(); LB(Bs[35],11,2,0); ZK(4); SB();
        MF(5,3,10,Br[1]); SB(); LB(Bs[35],11,2,1); SB();
        MF(5,0,9,Br[2]); SB(); LB(Bs[35],11,1,0); SB();
        MF(5,1,9,Br[2]); SB(); LB(Bs[35],11,1,1); SB();
        MF(5,2,9,Br[2]); SB(); LB(Bs[35],11,0,0); SB();
        MF(5,3,9,Br[2]); SB(); LB(Bs[35],11,0,1); SB();
        MF(5,0,8,Br[3]); SB();
        MF(5,1,8,Br[3]); SB();
        MF(5,2,8,Br[3]); SB();
        MF(5,3,8,Br[3]); SB();
        MF(5,0,0,Bs[35]); SB();
        MF(5,1,0,Bs[35]); SB();
        MF(5,2,0,Bs[35]); SB();
        MF(5,3,0,Bs[35]); SB(); LA(13,0); SB();
        MF(5,0,7,Br[4]); SB(); LA(13,1); SB();
        MF(5,1,7,Br[4]); SB(); LA(12,0); SB();
        MF(5,2,7,Br[4]); SB(); LA(12,1); SB();
        MF(5,3,7,Br[4]); SB(); LB(Bs[41],6,3,0); SB();
        MF(5,0,6,Br[5]); SB(); LB(Bs[41],6,3,1); SB();
        MF(5,1,6,Br[5]); SB(); LB(Bs[41],6,2,0); SB();
        MF(5,2,6,Br[5]); SB(); LB(Bs[41],6,2,1); SB();
        MF(5,3,6,Br[5]); SB(); LB(Bs[41],6,1,0); SB();
        MF0(6,0,13,Br[0]); SB(); LB(Bs[41],6,1,1); SB();
        MF0(6,1,13,Br[0]); SB(); LB(Bs[41],6,0,0); SB();
        MF0(6,2,13,Br[0]); SB(); LB(Bs[41],6,0,1); Z0(5); SB();
        MF0(6,3,13,Br[0]); SB(); LB(Bs[42],7,3,0); Z1(5); SB();
        MF(6,0,12,Br[1]); SB(); LB(Bs[42],7,3,1); Z2(5); SB();
        MF(6,1,12,Br[1]); SB(); LB(Bs[42],7,2,0); Z3(5); SB();
        MF(6,2,12,Br[1]); SB(); LB(Bs[42],7,2,1); Z4(5); SB();
        MF(6,3,12,Br[1]); SB(); Z5(5); ZL(5); SB();
        MF(6,0,11,Br[2]); SB(); Z6(5); SB();
        MF(6,1,11,Br[2]); SB(); LB(Bs[42],7,1,0); Z7(5); SB();
        MF(6,2,11,Br[2]); SB(); ZW(5); SB();
        MF(6,3,11,Br[2]); SB();
        MF(6,0,7,Bs[41]); SB(); ZF(5); SB();
        MF(6,1,7,Bs[41]); SB(); LB(Bs[42],7,1,1); SB();
        MF(6,2,7,Bs[41]); SB(); LB(Bs[42],7,0,0); ZK(5); SB();
        MF(6,3,7,Bs[41]); SB(); LB(Bs[42],7,0,1); SB();
        MF0(7,0,9,Bs[41]); SB();
        MF0(7,1,9,Bs[41]); SB();
        MF0(7,2,9,Bs[41]); SB();
        MF0(7,3,9,Bs[41]); SB();
        MF(6,0,6,Bs[42]); SB(); LB(Bs[44],8,3,0); SB();
        MF(6,1,6,Bs[42]); SB(); LB(Bs[44],8,3,1); SB();
        MF(6,2,6,Bs[42]); SB(); LB(Bs[44],8,2,0); SB();
        MF(6,3,6,Bs[42]); SB(); LB(Bs[44],8,2,1); SB();
        MF(7,0,8,Bs[42]); SB(); LB(Bs[44],8,1,0); SB();
        MF(7,1,8,Bs[42]); SB(); LB(Bs[44],8,1,1); SB();
        MF(7,2,8,Bs[42]); SB(); LB(Bs[44],8,0,0); SB();
        MF(7,3,8,Bs[42]); SB(); LB(Bs[44],8,0,1); SB();
        MF(6,0,10,Br[3]); SB(); LB(Bs[45],9,3,0); SB();
        MF(6,1,10,Br[3]); SB(); LB(Bs[45],9,3,1); SB();
        MF(6,2,10,Br[3]); SB(); LB(Bs[45],9,2,0); SB();
        MF(6,3,10,Br[3]); SB(); LB(Bs[45],9,2,1); SB();
        MF(6,0,5,Bs[44]); SB(); LB(Bs[45],9,1,0); SB();
        MF(6,1,5,Bs[44]); SB(); LB(Bs[45],9,1,1); SB();
        MF(6,2,5,Bs[44]); SB(); LB(Bs[45],9,0,0); SB();
        MF(6,3,5,Bs[44]); SB(); LB(Bs[45],9,0,1); SB();
        MF(7,0,7,Bs[44]); SB();
        MF(7,1,7,Bs[44]); SB();
        MF(7,2,7,Bs[44]); SB();
        MF(7,3,7,Bs[44]); SB();
        MF(6,0,4,Bs[45]); SB(); LB(Bs[47],10,3,0); SB();
        MF(6,1,4,Bs[45]); SB(); LB(Bs[47],10,3,1); SB();
        MF(6,2,4,Bs[45]); SB(); LB(Bs[47],10,2,0); SB();
        MF(6,3,4,Bs[45]); SB(); LB(Bs[47],10,2,1); SB();
        MF(7,0,6,Bs[45]); SB(); LB(Bs[47],10,1,0); SB();
        MF(7,1,6,Bs[45]); SB(); LB(Bs[47],10,1,1); SB();
        MF(7,2,6,Bs[45]); SB(); LB(Bs[47],10,0,0); SB();
        MF(7,3,6,Bs[45]); SB(); LB(Bs[47],10,0,1); SB();
        MF(6,0,9,Br[4]); SB(); LB(Bs[48],11,3,0); SB();
        MF(6,1,9,Br[4]); SB(); LB(Bs[48],11,3,1); SB();
        MF(6,2,9,Br[4]); SB(); LB(Bs[48],11,2,0); SB();
        MF(6,3,9,Br[4]); SB(); LB(Bs[48],11,2,1); SB();
        MF(6,0,3,Bs[47]); SB(); LB(Bs[48],11,1,0); SB();
        MF(6,1,3,Bs[47]); SB(); LB(Bs[48],11,1,1); SB();
        MF(6,2,3,Bs[47]); SB(); LB(Bs[48],11,0,0); SB();
        MF(6,3,3,Bs[47]); SB(); LB(Bs[48],11,0,1); SB();
        MF(7,0,5,Bs[47]); SB(); LB(Bs[50],12,3,0); SB();
        MF(7,1,5,Bs[47]); SB(); LB(Bs[50],12,3,1); SB();
        MF(7,2,5,Bs[47]); SB(); LB(Bs[50],12,2,0); SB();
        MF(7,3,5,Bs[47]); SB(); LB(Bs[50],12,2,1); SB();
        MF(6,0,2,Bs[48]); SB(); LB(Bs[50],12,1,0); SB();
        MF(6,1,2,Bs[48]); SB(); LB(Bs[50],12,1,1); SB();
        MF(6,2,2,Bs[48]); SB(); LB(Bs[50],12,0,0); SB();
        MF(6,3,2,Bs[48]); SB(); LB(Bs[50],12,0,1); SB();
        MF(7,0,4,Bs[48]); SB(); LB(Bs[51],13,3,0); SB();
        MF(7,1,4,Bs[48]); SB(); LB(Bs[51],13,3,1); SB();
        MF(7,2,4,Bs[48]); SB(); LB(Bs[51],13,2,0); SB();
        MF(7,3,4,Bs[48]); SB(); LB(Bs[51],13,2,1); SB();
        MF(6,0,8,Br[5]); SB(); LB(Bs[51],13,1,0); SB();
        MF(6,1,8,Br[5]); SB(); LB(Bs[51],13,1,1); SB();
        MF(6,2,8,Br[5]); SB(); LB(Bs[51],13,0,0); SB();
        MF(6,3,8,Br[5]); SB(); LB(Bs[51],13,0,1); SB();
        MF(6,0,1,Bs[50]); SB();
        MF(6,1,1,Bs[50]); SB();
        MF(6,2,1,Bs[50]); SB();
        MF(6,3,1,Bs[50]); SB();
        MF(6,0,0,Bs[51]); SB(); LA(15,0); SB();
        MF(6,1,0,Bs[51]); SB(); LA(15,1); SB();
        MF(6,2,0,Bs[51]); SB(); LB(Bs[53],14,3,0); SB();
        MF(6,3,0,Bs[51]); SB(); LB(Bs[53],14,3,1); SB();
        MF(7,0,3,Bs[50]); SB(); LB(Bs[53],14,2,0); SB();
        MF(7,1,3,Bs[50]); SB(); LB(Bs[53],14,2,1); SB();
        MF(7,2,3,Bs[50]); SB(); LB(Bs[53],14,1,0); Z0(6); SB();
        MF(7,3,3,Bs[50]); SB(); LB(Bs[53],14,1,1); Z1(6); SB();
        MF(7,0,2,Bs[51]); SB(); LB(Bs[53],14,0,0); Z2(6); SB();
        MF(7,1,2,Bs[51]); SB(); LB(Bs[53],14,0,1); Z3(6); SB();
        MF(7,2,2,Bs[51]); SB(); LA(14,0); Z4(6); SB();
        MF(7,3,2,Bs[51]); SB(); Z5(6); ZL(6); SB();
        MF(7,0,15,Br[0]); SB(); Z6(6); SB();
        MF(7,1,15,Br[0]); SB(); LA(14,1); Z7(6); SB();
        MF(7,2,15,Br[0]); SB(); ZW(6); SB();
        MF(7,3,15,Br[0]); SB();
        MF(7,0,1,Bs[53]); SB(); ZF(6); SB();
        MF(7,1,1,Bs[53]); SB();
        MF(7,2,1,Bs[53]); SB(); ZK(6); SB();
        MF(7,3,1,Bs[53]); SB();
        MF(7,0,14,Br[1]); SB(); LB(Bs[57],15,3,0); SB();
        MF(7,1,14,Br[1]); SB(); LB(Bs[57],15,3,1); SB();
        MF(7,2,14,Br[1]); SB(); LB(Bs[57],15,2,0); SB();
        MF(7,3,14,Br[1]); SB(); LB(Bs[57],15,2,1); SB();
        MF(7,0,13,Br[2]); SB(); LB(Bs[57],15,1,0); SB();
        MF(7,1,13,Br[2]); SB(); LB(Bs[57],15,1,1); SB();
        MF(7,2,13,Br[2]); SB(); LB(Bs[57],15,0,0); SB();
        MF(7,3,13,Br[2]); SB(); LB(Bs[57],15,0,1); SB();
        MF(7,0,12,Br[3]); SB();
        MF(7,1,12,Br[3]); SB();
        MF(7,2,12,Br[3]); SB();
        MF(7,3,12,Br[3]); SB();
        MF(7,0,0,Bs[57]); SB();
        MF(7,1,0,Bs[57]); SB();
        MF(7,2,0,Bs[57]); SB();
        MF(7,3,0,Bs[57]); SB();
        MF(7,0,11,Br[4]); SB();
        MF(7,1,11,Br[4]); SB();
        MF(7,2,11,Br[4]); SB();
        MF(7,3,11,Br[4]); SB();
        MF(7,0,10,Br[5]); SB();
        MF(7,1,10,Br[5]); SB();
        MF(7,2,10,Br[5]); SB();
        MF(7,3,10,Br[5]); SB();
        Z0(7); Z1(7); Z2(7); Z3(7); Z4(7); ZL(7); Z5(7); Z6(7); Z7(7); ZW(7); ZF(7); SB();
    } else {
        LAS char* fg = sm + FG_OFF + chalf * FGC + (4 * Jp + kb) * FGS + 4 * i16;
        LAS char* fa = sm + FG_OFF + chalf * FGC + i16 * FGR + 16 * kb;
        LAS char* fb = fa + 16 * Jp * FGR;
        unsigned sko = d0 * 4096 + 16 * t8, go = 16 * (lane & 31), gro = ((unsigned)blockIdx.x + 512 * (wv & 3) + 256 * hi) * 1024 + go, dof = 2 * d0;
        PIN(fg); PIN(fa); PIN(fb); PIN(sko); PIN(go); PIN(gro); PIN(dof);
        addr_inbox();
        *(LAS v2u*)(sm + F_OFF + (pp >> 1) * 1024 + (pp & 1) * 8) = (v2u){0u, 0u};
        v4u g_u0, g_u1, g_w0, g_w1, sk0, sk1;
        unsigned dsk_, dss_;
        auto load_extra = [&]() {
            SB();
            sk0 = *(const GAS v4u*)(p_ssmk + sko); sk1 = *(const GAS v4u*)(p_ssmk + sko + 4096);
            dsk_ = *(const GAS unsigned*)(p_dskip + dof); dss_ = *(const GAS unsigned*)(p_ssmD + dof);
            SB();
        };
        auto load_gate = [&]() {
            SB();
            g_u0 = *(const GAS v4u*)(p_u + gro); g_u1 = *(const GAS v4u*)(p_u + gro + 512);
            g_w0 = *(const GAS v4u*)(p_gw + go); g_w1 = *(const GAS v4u*)(p_gw + go + 512);
            int eo = WS_E; PIN(eo);
            tag = __builtin_amdgcn_raw_buffer_load_b32(wsr, eo, 0, 16);
            SB();
        };
        asm volatile("s_barrier" ::: "memory");
        __builtin_amdgcn_s_setprio(0);
        VMWAIT(4);
        asm volatile("" :: "v"(cv));
        asm volatile("" : "+v"(p_ldt), "+v"(p_lar), "+v"(p_aim));
        const float dt = rbf(exp_acc(__uint_as_float(p_ldt)));
        const float zr = rbf(exp_acc(__uint_as_float(p_lar))) * (-dt) * 1.44269504088896341f;
        const float zi = __uint_as_float(p_aim) * dt * 0.15915494309189535f;
        const cst e8 = cexp_p(zr, zi, 8.f), e64 = cexp_p(zr, zi, 64.f), e1 = cexp_p(zr, -zi, 1.f), e256 = cexp_p(zr, -zi, 256.f);
        VMWAIT(0);
        asm volatile("" : "+v"(p_cre), "+v"(p_cim), "+v"(m_re), "+v"(m_im));
        unsigned mx = max(max(m_re & 0x7fffu, (m_re >> 16) & 0x7fffu), max(m_im & 0x7fffu, (m_im >> 16) & 0x7fffu));
        mx = max(mx, dppu<0xB1, 0xf>(mx)); mx = max(mx, dppu<0x4E, 0xf>(mx)); mx = max(mx, dppu<0x141, 0xf>(mx)); mx = max(mx, dppu<0x140, 0xf>(mx));
        mx = max(mx, dppu<0x142, 0xa>(mx)); mx = max(mx, dppu<0x143, 0xc>(mx));
        int ex = __builtin_amdgcn_readlane((int)mx, 63) >> 7;
        ex = min(max(ex, 30), 230);
        const float cs = __uint_as_float((unsigned)(127 + 13 + 127 - ex) << 23);
        const float kscale = 2.f * dt * __uint_as_float((unsigned)(ex - 13 - 15) << 23);
#define PUT(row, v) *(LAS unsigned*)(fg + (row) * FGR) = pkh(v)
        v2f f[8], g[9];
        f[0] = (v2f){32768.f, 0.f}; f[1] = e8.b * 32768.f;
        g[0] = (v2f){__uint_as_float(p_cre) * cs, __uint_as_float(p_cim) * -cs};
#pragma unroll
        for (int b = 1; b < 9; ++b) { if (b > 1 && b < 8) f[b] = cmul(f[b - 1], e8); g[b] = cmul(g[b - 1], e1); }
#pragma unroll
        for (int b = 0; b < 8; ++b) { PUT(b, f[b]); }
#pragma unroll
        for (int b = 0; b < 8; ++b) { PUT(32 + b, g[b]); }
        v2f gA = cmul(g[0], e256), gB = cmul(g[4], e256), gC = cmul(g[8], e256);
        PUT(40, gA); PUT(41, cmul(g[2], e256)); PUT(42, gB); PUT(44, gB); PUT(45, cmul(g[6], e256)); PUT(46, gC);
        f[0] = e64.b * 32768.f;
#pragma unroll
        for (int z = 1; z < 4; ++z) {
#pragma unroll
            for (int b = 0; b < 8; ++b) { if (z > 1 || b > 0) f[b] = cmul(f[b], e64); PUT(8 * z + b, f[b]); }
            if (z < 3) { gA = cmul(gA, e256); gB = cmul(gB, e256); gC = cmul(gC, e256); PUT(44 + 4 * z, gA); PUT(45 + 4 * z, gB); PUT(46 + 4 * z, gC); }
            if (z == 2) load_gate();
        }
#pragma unroll
        for (int z = 0; z < 4; ++z) { gA = cmul(gA, e256); gC = cmul(gC, e256); PUT(56 + 2 * z, gA); PUT(57 + 2 * z, gC); }
        {
            float acc = 0.f, acc2 = 0.f;
#pragma unroll
            for (int e = 0; e < 4; ++e) { acc = dot2(g_u0[e], g_w0[e], acc); acc2 = dot2(g_u1[e], g_w1[e], acc2); }
            acc += acc2;
            acc += dppf<0xB1, 0xf>(acc); acc += dppf<0x4E, 0xf>(acc); acc += dppf<0x141, 0xf>(acc); acc += dppf<0x140, 0xf>(acc);
            acc += dppf<0x142, 0xa>(acc);
            const float gval = __builtin_amdgcn_rcpf(1.f + __builtin_amdgcn_exp2f(rbf(acc) * -1.44269504088896341f));
            if ((lane & 31) == 31) *(volatile LAS u16*)(sm + S_OFF + 16 + 4 * (wv & 3) + 2 * hi) = (u16)pkbf(gval, gval);
            tag = tag * 0x9E3779B1u + 0x7F4A7C15u;
            asm volatile("" : "+v"(tag) :: "memory");
            *(volatile LAS unsigned*)(sm + S_OFF + 32 + 4 * wv) = 1u;
            const v4u ar = *(volatile LAS v4u*)(sm + S_OFF + 48);
            const v2u gg = *(volatile LAS v2u*)(sm + S_OFF + 16 + 8 * (lane & 1));
            __builtin_amdgcn_raw_buffer_store_b128((v4u){gg.x, tag, gg.y, tag}, wsr,
                (lane < 2 && (ar.x & ar.y & ar.z & ar.w)) ? WS_G + 32 * (int)blockIdx.x + 16 * lane : -16, 0, 16);
            SB();
        }
        BAR();
        load_extra();
        LAS char *kw0, *kw1;
        unsigned sa, sb, sc, sd;
        v8h fb_[8], g0_[8], g1_[8];
#pragma unroll
        for (int ks = 0; ks < 8; ++ks) { fb_[ks] = *(LAS v8h*)(fb + FGS * ks); g0_[ks] = *(LAS v8h*)(fa + FGS * ks + 32 * FGR); g1_[ks] = *(LAS v8h*)(fa + FGS * ks + 48 * FGR); }
        SB();
        LAS char* kw = sm + K_OFF + 2 * chalf * KS + 32 + 16 * (16 * Jp + i16);
        kw0 = kw + 512 * hi + 8 * rp;
        kw1 = kw + 1024 + 512 * (kb + (kb == 3));
        sa = hi ? 0x01000100u : 0x03020100u; sd = hi ? 0x05040302u : 0x07060504u;
        sb = (hi && !(i16 & 1)) ? 0x03020100u : 0x03020302u; sc = hi ? 0x03020302u : 0x05040504u;
        v4f ac0 = {0.f, 0.f, 0.f, 0.f}, ac1 = {0.f, 0.f, 0.f, 0.f};
#pragma unroll
        for (int ks = 0; ks < 8; ++ks) {
            ac0 = __builtin_amdgcn_mfma_f32_16x16x32_f16(g0_[ks], fb_[ks], ac0, 0, 0, 0);
            ac1 = __builtin_amdgcn_mfma_f32_16x16x32_f16(g1_[ks], fb_[ks], ac1, 0, 0, 0);
        }
        ac0 *= kscale; ac1 *= kscale;
        const unsigned v0 = pkbf(ac0[0], ac0[1]), v1 = pkbf(ac0[2], ac0[3]), w0 = pkbf(ac1[0], ac1[1]), w1 = pkbf(ac1[2], ac1[3]);
        const unsigned d22 = __builtin_amdgcn_perm(w1, w1, 0x01000100u);
        const v2u o0 = {__builtin_amdgcn_perm(v1, v0, sa), __builtin_amdgcn_perm(v1, v0, sd)};
        const v4u o1 = {__builtin_amdgcn_perm(w1, w0, 0x01000100u), __builtin_amdgcn_perm(w1, w0, sa), __builtin_amdgcn_perm(w1, w0, sb), __builtin_amdgcn_perm(w1, w0, sc)};
        v4u o2 = {d22, d22, __builtin_amdgcn_perm(w1, w1, sb), __builtin_amdgcn_perm(w1, w1, 0x03020302u)};
        PIN(o2);
        SB();
        *(LAS v2u*)kw0 = o0;
        *(LAS v4u*)kw1 = o1;
        if (hi) *(LAS v4u*)(kw1 + 512) = o2;
        {
            LAS char* skw = sm + K_OFF + KS + 32 + 16 * t8;
            *(LAS v4u*)skw = sk0; *(LAS v4u*)(skw + 2 * KS) = sk1;
        }
        asm volatile("" ::: "memory");
        *(volatile LAS unsigned*)fw = 1u;
        SB();
        int gi = WS_G + 32 * t8;
        v4u gq0, gq1;
        const int Q0 = t8 >> 2, Q1 = (t8 - 3) >> 2;
        LAS char* y0 = sm + Y_OFF + 64 + 16 * (Q0 ^ ((Q0 >> 3) & 3)) + 4 * (t8 & 3);
        LAS char* y1 = sm + Y_OFF + YS + 64 + 16 * (Q1 ^ ((Q1 >> 3) & 3)) + 4 * ((t8 - 3) & 3);
        LAS char* xe = sm + X_OFF + (t8 & 3) * XR + 128 + 2 * Q0;
        int oo = t8 * 1024 + 2 * d0;
        PIN(y0); PIN(y1); PIN(xe); PIN(oo); PIN(gi);
        SB();
#define GLOAD() do { gq0 = __builtin_amdgcn_raw_buffer_load_b128(wsr, gi, 0, 16); gq1 = __builtin_amdgcn_raw_buffer_load_b128(wsr, gi + 16, 0, 16); } while (0)
#define GFRESH() __all((((gq0.y ^ tag) | (gq0.w ^ tag)) | ((gq1.y ^ tag) | (gq1.w ^ tag))) == 0)
        GLOAD();
        for (int z = 0; z < EDEL; ++z) __builtin_amdgcn_s_sleep(2);
        POLL();
        SB();
        for (unsigned spins = 0; !GFRESH() && spins < (1u << 16); ++spins) { __builtin_amdgcn_s_sleep(8); asm volatile("" ::: "memory"); GLOAD(); }
        READY();
        INBOX();
        SB();
        __builtin_amdgcn_s_setprio(1);
        const rsrc_t outr = __builtin_amdgcn_make_buffer_rsrc((void*)mk64(kB.s0, kB.s1), 0, (int)kB.s2, (int)kB.s3);
        const float dk0 = bflo(dsk_), dk1 = bfhi(dsk_), ds0 = bflo(dss_), ds1 = bfhi(dss_);
        float eu0 = 0.f, ek0 = 0.f, eu1 = 0.f, ek1 = 0.f;
        unsigned seen = 0;
#pragma unroll
        for (int t = 0; t < 8; ++t) {
            if (t == 1) {
                TAKE();
                asm volatile("" ::: "memory");
                *(volatile LAS unsigned*)fw = 3u;
                SB();
            }
            if (t == 5 && blockIdx.x == 0 && wv == 4) {
                for (unsigned spins = 0; spins < (1u << 16); ++spins) {
                    asm volatile("" ::: "memory");
                    unsigned ok = 1;
#pragma unroll
                    for (int z = 0; z < 4; ++z) ok &= __builtin_amdgcn_raw_buffer_load_b32(wsr, WS_F + lane * 128, 8192 * z, 16) == tag;
                    if (__all(ok)) break;
                }
                if (lane == 0) __builtin_amdgcn_raw_buffer_store_b32(tag, wsr, WS_E, 0, 16);
            }
            if (seen <= (unsigned)(t + 1)) {
                for (;;) {
                    const v4u f = *(volatile LAS v4u*)(sm + F_OFF);
                    seen = __builtin_amdgcn_readfirstlane(min(min(f.x, f.y), min(f.z, f.w)));
                    if (seen > (unsigned)(t + 1)) break;
                    __builtin_amdgcn_s_sleep(1);
                }
            }
            if (t >= 6) __builtin_amdgcn_s_setprio(3);
            v2f la, lb, wa, wb;
            asm volatile("ds_read2st64_b32 %0, %8 offset0:%c11 offset1:%c12\n\tds_read2st64_b32 %1, %8 offset0:%c13 offset1:%c14\n\t"
                         "ds_read_u16_d16_hi %4, %10 offset:%c15\n\tds_read_u16_d16_hi %5, %10 offset:%c16\n\t"
                         "ds_read_u16_d16_hi %6, %10 offset:%c17\n\tds_read_u16_d16_hi %7, %10 offset:%c18\n\t"
                         "ds_read2st64_b32 %2, %9 offset0:%c11 offset1:%c12\n\tds_read2st64_b32 %3, %9 offset0:%c13 offset1:%c14\n\t"
                         "s_waitcnt lgkmcnt(0)"
                         : "=&v"(la), "=&v"(lb), "=&v"(wa), "=&v"(wb), "+v"(eu0), "+v"(ek0), "+v"(eu1), "+v"(ek1)
                         : "v"(y0), "v"(y1), "v"(xe), "n"(4 * t), "n"(4 * t + 2 * YS / 256), "n"(4 * t + 4 * YS / 256), "n"(4 * t + 6 * YS / 256),
                           "n"(128 * t), "n"(128 * t + XC), "n"(128 * t + 2 * XC), "n"(128 * t + 3 * XC)
                         : "memory");
            const unsigned gp = t < 4 ? (t < 2 ? gq0.x : gq0.z) : (t < 6 ? gq1.x : gq1.z);
            const float g = (t & 1) ? bfhi(gp) : bflo(gp);
            float a0 = __builtin_fmaf(eu0, dk0, la.x), b0 = __builtin_fmaf(ek0, ds0, la.y), a1 = __builtin_fmaf(eu1, dk1, lb.x), b1 = __builtin_fmaf(ek1, ds1, lb.y);
            PIN(a0); PIN(b0); PIN(a1); PIN(b1);
            a0 += wa.x; PIN(a0); b0 += wa.y; PIN(b0); a1 += wb.x; PIN(a1); b1 += wb.y; PIN(b1);
            b0 *= eu0; PIN(b0); b1 *= eu1; PIN(b1);
            a0 -= b0; PIN(a0); a1 -= b1; PIN(a1);
            float c00 = __builtin_fmaf(g, a0, b0); PIN(c00);
            float c01 = __builtin_fmaf(g, a1, b1); PIN(c01);
            __builtin_amdgcn_raw_buffer_store_b32(pkbf(c00, c01), outr, oo, t * 262144, 0);
            if (t >= 6) __builtin_amdgcn_s_setprio(1);
            SB();
        }
    }
    };
    if (wv >= 4) body(std::integral_constant<bool, false>{});
    else body(std::integral_constant<bool, true>{});
}

extern "C" void kernel_launch(void* const* d_in, const int* in_sizes, int n_in, void* d_out, int out_size, void* d_ws,
                              size_t ws_size, hipStream_t stream) {
    (void)hipFuncSetAttribute((const void*)fused_kernel, hipFuncAttributeMaxDynamicSharedMemorySize, LDS_BYTES);
    Args a;
    a.lar = (const u16*)d_in[1]; a.aim = (const u16*)d_in[2]; a.cre = (const u16*)d_in[3]; a.cim = (const u16*)d_in[4];
    a.ldt = (const u16*)d_in[5]; a.dskip = (const u16*)d_in[6]; a.shk = (const u16*)d_in[10];
    a.ssmk = (const u16*)d_in[11]; a.ssmD = (const u16*)d_in[12]; a.gw = (const u16*)d_in[13];
    a.ub = Buf{d_in[0], 2048 * 1024, 0x00020000}; a.vb = Buf{d_in[9], 2048 * 1024, 0x00020000};
    a.kk = Buf{d_in[8], 2048 * 1024, 0x00020000}; a.ws = Buf{d_ws, WS_E + 64, 0x00020000}; a.out = Buf{d_out, 2048 * 1024, 0x00020000};
    fused_kernel<<<256, 512, LDS_BYTES, stream>>>(a);
}
```
